```python
import torch

def _softmax(x, axis=-1):
    return torch.softmax(x, dim=axis)

def _gelu(x):
    return torch.nn.functional.gelu(x, approximate='tanh')

def _silu(x):
    return torch.nn.functional.silu(x)

def _sigmoid(x):
    return torch.sigmoid(x)

def _layernorm(x, axis=-1, eps=1e-05):
    m = x.mean(dim=axis, keepdim=True)
    v = x.var(dim=axis, keepdim=True, unbiased=False)
    return (x - m) / torch.sqrt(v + eps)

def _scatter_add(tgt, idx, src, axis=0):
    return tgt.clone().index_add_(axis, idx, src)

def _segment_sum(src, seg, n):
    out = torch.zeros((n,) + src.shape[1:], dtype=src.dtype)
    return out.index_add_(0, seg, src)

def _pad(x, pw):
    flat = tuple((v for pair in reversed(pw) for v in pair))
    return torch.nn.functional.pad(x, flat)
kernel_name = 'adaLN_cond__ssm_conv_s4__glu_gated_bilinear__prompt'

def setup_inputs():

    _w = lambda a: (lambda t: t.to(torch.bfloat16) if t.is_floating_point() else t)(torch.as_tensor(a))

    def _A_setup():
        B, L, D, N = (1, 2048, 576, 128)
        A_re = -(lambda _x: torch.exp(torch.as_tensor(_x)))((lambda *s: torch.randn(*s, dtype=torch.bfloat16) * 3.2)(D, N))
        A_im = torch.pi * (lambda *s: torch.randn(*s, dtype=torch.bfloat16) * 0.05)(D, N)
        B_re = (lambda *s: torch.randn(*s, dtype=torch.bfloat16) * 0.1)(D, N)
        B_im = (lambda *s: torch.randn(*s, dtype=torch.bfloat16) * 0.1)(D, N)
        C_re = (lambda *s: torch.randn(*s, dtype=torch.bfloat16) * 0.1)(D, N)
        C_im = (lambda *s: torch.randn(*s, dtype=torch.bfloat16) * 0.1)(D, N)
        log_dt = (lambda *s: torch.randn(*s, dtype=torch.bfloat16) * 0.05)(D) * 0.5 - 4.0
        Dskip = (lambda *s: torch.randn(*s, dtype=torch.bfloat16) * 0.05)(D)
        return {'u': _w((lambda *s: torch.randn(*s, dtype=torch.bfloat16) * 0.05)(B, L, D)), 'A_re': _w(A_re), 'A_im': _w(A_im), 'B_re': _w(B_re), 'B_im': _w(B_im), 'C_re': _w(C_re), 'C_im': _w(C_im), 'log_dt': _w(log_dt), 'Dskip': _w(Dskip)}
    _A_in = _A_setup()

    def _B_setup():
        B, S, D, F = (1, 2048, 576, 4608)
        return {'x': _w((lambda *s: torch.randn(*s, dtype=torch.bfloat16) * 0.05)(B, S, D)), 'w_gate': _w((lambda *s: torch.randn(*s, dtype=torch.bfloat16) * 0.8)(D, F) * D ** (-0.5)), 'w_up': _w((lambda *s: torch.randn(*s, dtype=torch.bfloat16) * 0.8)(D, F) * D ** (-0.5)), 'w_down': _w((lambda *s: torch.randn(*s, dtype=torch.bfloat16) * 0.8)(F, D) * F ** (-0.5))}
    _B_in = _B_setup()
    return {'A_u': _A_in['u'], 'A_A_re': _A_in['A_re'], 'A_A_im': _A_in['A_im'], 'A_B_re': _A_in['B_re'], 'A_B_im': _A_in['B_im'], 'A_C_re': _A_in['C_re'], 'A_C_im': _A_in['C_im'], 'A_log_dt': _A_in['log_dt'], 'A_Dskip': _A_in['Dskip'], 'B_x': _B_in['x'], 'B_w_gate': _B_in['w_gate'], 'B_w_up': _B_in['w_up'], 'B_w_down': _B_in['w_down'], 'c': _w((lambda *s: torch.randn(*s, dtype=torch.bfloat16) * 0.2)(1, 576) * 8.0), 'w_mod': _w((lambda *s: torch.randn(*s, dtype=torch.bfloat16) * 0.2)(576, 6 * 576) * 576 ** (-0.5) * 8.0)}

def reference(A_u, A_A_re, A_A_im, A_B_re, A_B_im, A_C_re, A_C_im, A_log_dt, A_Dskip, B_x, B_w_gate, B_w_up, B_w_down, c, w_mod):

    def _A_ref(u, A_re, A_im, B_re, B_im, C_re, C_im, log_dt, Dskip):
        Bsz, L, D = u.shape
        dt = torch.exp(log_dt)[:, None]
        A = A_re + 1j * A_im
        Bc = B_re + 1j * B_im
        Cc = C_re + 1j * C_im
        logdA = dt * A
        dB = dt * Bc
        l = (lambda x, dt: x.to(dt))(torch.arange(L), A_re.dtype)
        P = torch.exp(logdA[:, :, None] * l[None, None, :])
        K = torch.einsum('dn,dnl,dn->dl', Cc, P, dB).real
        L2 = 2 * L
        Kp = _pad(K, ((0, 0), (0, L)))
        up = (lambda x, *d: x.permute(*d))(u, 0, 2, 1)
        up = _pad(up, ((0, 0), (0, 0), (0, L)))
        Kf = (lambda x, axes=(-2, -1): torch.fft.rfft2(x.float(), dim=axes))(Kp[:, None, :], axes=(-2, -1))
        Uf = (lambda x, axes=(-2, -1): torch.fft.rfft2(x.float(), dim=axes))(up[:, :, None, :], axes=(-2, -1))
        Yf = Uf * Kf[None, :, :, :]
        y = (lambda x, s, axes=(-2, -1): torch.fft.irfft2(x, s=s, dim=axes).to(torch.bfloat16))(Yf, (1, L2), axes=(-2, -1))[:, :, 0, :L]
        y = (lambda x, *d: x.permute(*d))(y, 0, 2, 1) + u * Dskip[None, None, :]
        return y

    def _B_ref(x, w_gate, w_up, w_down):
        g = torch.einsum('bsd,df->bsf', x, w_gate)
        u = torch.einsum('bsd,df->bsf', x, w_up)
        h = g * u
        return torch.einsum('bsf,fd->bsd', h, w_down)
    x = A_u
    mod = (_silu(c) @ w_mod).reshape(c.shape[0], 6, 1, -1)
    sa, ca, ga, sb, cb, gb = (mod[:, 0].clone(), mod[:, 1].clone(), mod[:, 2].clone(), mod[:, 3].clone(), mod[:, 4].clone(), mod[:, 5].clone())
    h = _layernorm(x, axis=-1) * (1 + ca) + sa
    x = x + ga * _A_ref(h, A_A_re, A_A_im, A_B_re, A_B_im, A_C_re, A_C_im, A_log_dt, A_Dskip)
    h = _layernorm(x, axis=-1) * (1 + cb) + sb
    x = x + gb * _B_ref(h, B_w_gate, B_w_up, B_w_down)
    return x
```

```cpp
#include <hip/hip_runtime.h>
#include <stdint.h>

typedef unsigned short u16;
typedef uint32_t u32;
typedef __attribute__((ext_vector_type(8))) __bf16 bf16x8;
typedef __attribute__((ext_vector_type(2))) __bf16 bf16x2;
typedef __attribute__((ext_vector_type(4))) short s16x4;
typedef __attribute__((ext_vector_type(2))) float f32x2;
typedef __attribute__((ext_vector_type(4))) float f32x4;
typedef __attribute__((ext_vector_type(16))) float f32x16;
typedef __attribute__((ext_vector_type(2))) u32 u32x2;
typedef __attribute__((ext_vector_type(4))) u32 u32x4;
typedef __amdgpu_buffer_rsrc_t rsrc_t;
#define LDSP __attribute__((address_space(3)))
#define DEV __device__ __forceinline__

constexpr int L_ = 2048, D_ = 576, F_ = 4608;

constexpr u32 WS_FLAG = 0;
constexpr u32 WS_MODP = 32768;
constexpr u32 WS_MODF = WS_MODP + 2 * 3456 * 4;
constexpr u32 WS_ST1 = 81920;
constexpr u32 WS_CSB = 98304;
constexpr u32 WS_XT4 = 1u << 20;
constexpr u32 WS_X1T = WS_XT4 + 2359296;
constexpr u32 WS_KC = WS_X1T + 2359296;
constexpr u32 WS_WP = WS_KC + 576 * 8320;
constexpr u32 WS_HT = WS_WP + 32 * 324 * 1024;
constexpr u32 WS_XCH = WS_HT + 64 * 288 * 1024;
constexpr u32 WS_END = WS_XCH + 256 * 4 * 2 * 4608;

DEV float lo2f(u32 v) { return __uint_as_float(v << 16); }
DEV float hi2f(u32 v) { return __uint_as_float(v & 0xffff0000u); }
DEV float b2f(u16 v) { return __uint_as_float(((u32)v) << 16); }
DEV u32 pk2(float a, float b) { bf16x2 r = {(__bf16)a, (__bf16)b}; return __builtin_bit_cast(u32, r); }
DEV float rb(float a) { return (float)(__bf16)a; }
DEV rsrc_t mkrs(const void* p, u32 bytes) { return __builtin_amdgcn_make_buffer_rsrc((void*)p, 0, (int)bytes, 0x00020000); }
DEV void st16(rsrc_t rs, u32 off, u32x4 v) { __builtin_amdgcn_raw_buffer_store_b128(v, rs, off, 0, 16); }
DEV void st8(rsrc_t rs, u32 off, u32x2 v) { __builtin_amdgcn_raw_buffer_store_b64(v, rs, off, 0, 16); }
DEV void st4(rsrc_t rs, u32 off, u32 v) { __builtin_amdgcn_raw_buffer_store_b32(v, rs, off, 0, 16); }
DEV u32x4 ld16(rsrc_t rs, u32 voff, u32 soff) { return __builtin_amdgcn_raw_buffer_load_b128(rs, voff, soff, 0); }
DEV u32x2 ld8(rsrc_t rs, u32 voff, u32 soff) { return __builtin_amdgcn_raw_buffer_load_b64(rs, voff, soff, 0); }
DEV u32 ld4(rsrc_t rs, u32 voff, u32 soff) { return __builtin_amdgcn_raw_buffer_load_b32(rs, voff, soff, 0); }
DEV void dma16(rsrc_t rs, LDSP char* dst, u32 voff, u32 soff) {
  __builtin_amdgcn_raw_ptr_buffer_load_lds(rs, (LDSP void*)dst, 16, voff, soff, 0, 0);
}
#define WAIT_V(n) asm volatile("s_waitcnt vmcnt(" #n ")" ::: "memory")
#define WAIT_L(n) asm volatile("s_waitcnt lgkmcnt(" #n ")" ::: "memory")
#define BAR() do { __builtin_amdgcn_s_barrier(); asm volatile("" ::: "memory"); } while (0)
#define SCHED() __builtin_amdgcn_sched_barrier(0)
#define MFMA32(a, b, c) __builtin_amdgcn_mfma_f32_32x32x16_bf16(a, b, c, 0, 0, 0)
#define MFMA16(a, b, c) __builtin_amdgcn_mfma_f32_16x16x32_bf16(a, b, c, 0, 0, 0)
DEV bf16x8 asb(u32x4 v) { return __builtin_bit_cast(bf16x8, v); }

#define STAMP_BEGIN()
#define STAMP_END(k)

struct cplx { float r, i; };
DEV cplx cmul(cplx a, cplx b) { return {a.r * b.r - a.i * b.i, a.r * b.i + a.i * b.r}; }
DEV cplx cexpm(float zr2, float zrev, float m) {
  float e = __builtin_amdgcn_exp2f(zr2 * m);
  float f = __builtin_amdgcn_fractf(zrev * m);
  return {e * __builtin_amdgcn_cosf(f), e * __builtin_amdgcn_sinf(f)};
}

constexpr int KG_PITCH = 528, KG_CH = 53 * KG_PITCH;
constexpr int KG_TAB = 4 * KG_CH;
__global__ __launch_bounds__(512, 2) void k1(const u16* __restrict__ x, const u16* __restrict__ Are, const u16* __restrict__ Aim,
                                             const u16* __restrict__ Bre, const u16* __restrict__ Bim, const u16* __restrict__ Cre,
                                             const u16* __restrict__ Cim, const u16* __restrict__ ldt, const u16* __restrict__ cvec,
                                             const u16* __restrict__ wmod, char* ws) {
  __shared__ __attribute__((aligned(16))) char smem[KG_TAB + 4 * 4112];
  STAMP_BEGIN();
  const int b = blockIdx.x, tid = threadIdx.x, lane = tid & 63, w = tid >> 6;
  const rsrc_t rw = mkrs(ws, WS_END);
  if (b < 144) {
    const int ch = 4 * b + (w >> 1), n = (w & 1) * 64 + lane;
    const int pi = ch * 128 + n;
    const float are = b2f(Are[pi]), aim = b2f(Aim[pi]), bre = b2f(Bre[pi]), bim = b2f(Bim[pi]), cre = b2f(Cre[pi]), cim = b2f(Cim[pi]);
    const float ld = b2f(ldt[ch]);
    if (b < 128) {
      const int tok = 16 * b + 2 * w + (lane >> 5), hl = lane & 31;
      u32x4 v[3];
      const bool act = hl < 24;
      const u32x4* xr = (const u32x4*)(x + tok * D_);
#pragma unroll
      for (int i = 0; i < 3; i++) v[i] = act ? xr[hl + 24 * i] : (u32x4){0, 0, 0, 0};
      u32x2 ta[3], tb[3];
      const int pr = lane & 7, cg = lane >> 3;
#pragma unroll
      for (int it = 0; it < 3; it++) {
        const int c4 = 18 * w + cg + 8 * it;
        if (it < 2 || cg < 2) {
          ta[it] = *(const u32x2*)(x + (16 * b + 2 * pr) * D_ + 4 * c4);
          tb[it] = *(const u32x2*)(x + (16 * b + 2 * pr + 1) * D_ + 4 * c4);
        }
      }
      float s = 0.f;
#pragma unroll
      for (int i = 0; i < 3; i++)
#pragma unroll
        for (int j = 0; j < 4; j++) s += lo2f(v[i][j]) + hi2f(v[i][j]);
#pragma unroll
      for (int o = 16; o; o >>= 1) s += __shfl_xor(s, o);
      const float mean = s * (1.f / D_);
      float q = 0.f;
#pragma unroll
      for (int i = 0; i < 3; i++)
#pragma unroll
        for (int j = 0; j < 4; j++) {
          float e0 = lo2f(v[i][j]) - mean, e1 = hi2f(v[i][j]) - mean;
          q += e0 * e0 + e1 * e1;
        }
      if (!act) q = 0.f;
#pragma unroll
      for (int o = 16; o; o >>= 1) q += __shfl_xor(q, o);
      const float sd = sqrtf(rb(rb(q * (1.f / D_)) + 1e-5f));
      if (hl == 0) st4(rw, WS_ST1 + tok * 4, pk2(sd, mean));
#pragma unroll
      for (int it = 0; it < 3; it++) {
        const int c4 = 18 * w + cg + 8 * it;
        if (it < 2 || cg < 2) st16(rw, WS_XT4 + c4 * 16384 + (16 * b + 2 * pr) * 8, (u32x4){ta[it][0], ta[it][1], tb[it][0], tb[it][1]});
      }
    }
    const float dt = rb(__expf(ld));
    const float zr2 = dt * are * 1.44269504088896341f, zrev = dt * aim * 0.15915494309189535f;
    cplx g = cmul({cre, cim}, {dt * bre, dt * bim});
    const cplx w1 = cexpm(zr2, zrev, 1.f), w8 = cexpm(zr2, zrev, 8.f), w256 = cexpm(zr2, zrev, 256.f);
    LDSP char* cb = (LDSP char*)smem + (w >> 1) * KG_CH;
    LDSP char* pw = cb + n * 4;
#define PUTP(row, v) *(LDSP u32*)(pw + (row) * KG_PITCH) = pk2((v).r, -(v).i)
#define PUTQ(row, v) *(LDSP u32*)(pw + (21 + (row)) * KG_PITCH) = pk2((v).r, (v).i)
    {
      cplx p[8];
      p[0] = g;
#pragma unroll
      for (int a = 1; a < 8; a++) p[a] = cmul(p[a - 1], w1);
#pragma unroll
      for (int a = 0; a < 8; a++) PUTP(a, p[a]);
      cplx r0 = cmul(p[0], w256), r2 = cmul(p[2], w256), r4 = cmul(p[4], w256), r6 = cmul(p[6], w256);
      PUTP(8, r0); PUTP(9, r2); PUTP(10, r4); PUTP(11, r6);
      r0 = cmul(r0, w256); r4 = cmul(r4, w256);
      PUTP(12, r0); PUTP(13, r4);
      r0 = cmul(r0, w256); r4 = cmul(r4, w256);
      PUTP(14, r0); PUTP(15, r4);
#pragma unroll
      for (int a = 16; a < 21; a++) { r0 = cmul(r0, w256); PUTP(a, r0); }
      cplx q0 = {1.f, 0.f}, q1 = w8;
      const cplx w16 = cmul(w8, w8);
#pragma unroll
      for (int a = 0; a < 32; a += 2) {
        PUTQ(a, q0); PUTQ(a + 1, q1);
        q0 = cmul(q0, w16); q1 = cmul(q1, w16);
      }
    }
    __syncthreads();
    {
      const int r16 = lane & 15, q = lane >> 4, ct = w & 1;
      f32x4 a0 = {0, 0, 0, 0}, a1 = {0, 0, 0, 0};
      LDSP char* pa = cb + r16 * KG_PITCH + q * 16;
      LDSP char* pb = cb + (21 + 16 * ct + r16) * KG_PITCH + q * 16;
#pragma unroll
      for (int ks = 0; ks < 8; ks++) {
        bf16x8 fb = *(LDSP bf16x8*)(pb + ks * 64);
        bf16x8 f0 = *(LDSP bf16x8*)(pa + ks * 64);
        bf16x8 f1 = *(LDSP bf16x8*)(pa + 16 * KG_PITCH + ks * 64);
        a0 = MFMA16(f0, fb, a0);
        a1 = MFMA16(f1, fb, a1);
      }
      LDSP u16* tab = (LDSP u16*)((LDSP char*)smem + KG_TAB + (w >> 1) * 4112);
      const int bb = 8 * (16 * ct + r16);
      const u32 p01 = pk2(a0[0], a0[1]), p23 = pk2(a0[2], a0[3]), s01 = pk2(a1[0], a1[1]), s23 = pk2(a1[2], a1[3]);
      const int base = q == 0 ? 0 : q == 1 ? 4 : q == 2 ? 256 : 512;
      const int d1 = q < 2 ? 1 : q == 2 ? 2 : 4, d2 = q < 2 ? 2 : q == 2 ? 4 : 256;
      tab[bb + base] = (u16)p01;
      tab[bb + base + d1] = (u16)(p01 >> 16);
      tab[bb + base + d2] = (u16)p23;
      tab[bb + base + d2 + d1] = (u16)(p23 >> 16);
      if (q == 0) {
        tab[bb + 1024] = (u16)s01;
        tab[bb + 1280] = (u16)(s01 >> 16);
        tab[bb + 1536] = (u16)s23;
        tab[bb + 1792] = (u16)(s23 >> 16);
      } else if (q == 1 && bb == 0) {
        tab[2048] = (u16)s01;
      }
    }
    __syncthreads();
    {
      const LDSP u16* tab = (const LDSP u16*)((LDSP char*)smem + KG_TAB + (w >> 1) * 4112);
      const u32 ob = WS_KC + ch * 8320;
#pragma unroll
      for (int hf = 0; hf < 2; hf++) {
        const int l0 = 1024 * hf + 8 * n;
        u32 e[9];
#pragma unroll
        for (int j = 0; j < 9; j++) e[j] = tab[(int)rb((float)(l0 + j))];
        u32x4 c0 = {e[0] | e[1] << 16, e[2] | e[3] << 16, e[4] | e[5] << 16, e[6] | e[7] << 16};
        u32x4 c1 = {e[1] | e[2] << 16, e[3] | e[4] << 16, e[5] | e[6] << 16, e[7] | e[8] << 16};
        st16(rw, ob + 32 + l0 * 2, c0);
        st16(rw, ob + 4160 + 32 + l0 * 2, c1);
      }
      if (n < 4) {
        u32x4 z = {0, 0, 0, n == 3 ? ((u32)tab[0]) << 16 : 0u};
        st16(rw, ob + (n >> 1) * 4160 + (n & 1) * 16, z);
      }
    }
  } else if (b < 252) {
    const int t = b - 144, cbk = t >> 1, part = t & 1;
    const int rr = tid >> 3, cc = tid & 7;
    u32x4 v[5];
    float sc[5];
#pragma unroll
    for (int i = 0; i < 5; i++) {
      const int k = 288 * part + rr + 64 * i;
      const bool ok = i < 4 || rr < 32;
      v[i] = ok ? *(const u32x4*)(wmod + k * 3456 + cbk * 64 + cc * 8) : (u32x4){0, 0, 0, 0};
      sc[i] = ok ? b2f(cvec[k]) : 0.f;
    }
    float acc[8] = {0, 0, 0, 0, 0, 0, 0, 0};
#pragma unroll
    for (int i = 0; i < 5; i++) {
      const float s = rb(sc[i] / (1.f + __expf(-sc[i])));
#pragma unroll
      for (int j = 0; j < 4; j++) {
        acc[2 * j] += s * lo2f(v[i][j]);
        acc[2 * j + 1] += s * hi2f(v[i][j]);
      }
    }
#pragma unroll
    for (int j = 0; j < 8; j++) {
      acc[j] += __shfl_xor(acc[j], 8);
      acc[j] += __shfl_xor(acc[j], 16);
      acc[j] += __shfl_xor(acc[j], 32);
    }
    LDSP float* red = (LDSP float*)smem;
    if (lane < 8) {
      *(LDSP f32x4*)(red + w * 64 + lane * 8) = (f32x4){acc[0], acc[1], acc[2], acc[3]};
      *(LDSP f32x4*)(red + w * 64 + lane * 8 + 4) = (f32x4){acc[4], acc[5], acc[6], acc[7]};
    }
    __syncthreads();
    if (tid < 64) {
      float s = 0.f;
#pragma unroll
      for (int i = 0; i < 8; i++) s += red[i * 64 + tid];
      st4(rw, WS_MODP + (part * 3456 + cbk * 64 + tid) * 4, __float_as_uint(s));
    }
  } else if (b == 252) {
    if (tid < 256) st4(rw, WS_FLAG + tid * 128, 0u);
  }
  STAMP_END(0);
}

constexpr int C_KC = 0, C_HR = 33280, C_HP = 4608, C_Y = C_HR + 4 * C_HP;
__global__ __launch_bounds__(512, 2) void k2(const u16* __restrict__ dsk, const u16* __restrict__ wg, const u16* __restrict__ wu,
                                             char* ws) {
  __shared__ __attribute__((aligned(16))) char smem[C_Y + 65536];
  STAMP_BEGIN();
  const int b = blockIdx.x, tid = threadIdx.x, lane = tid & 63, w = __builtin_amdgcn_readfirstlane(tid >> 6);
  const rsrc_t rw = mkrs(ws, WS_END);
  const float* modp = (const float*)(ws + WS_MODP);
  LDSP char* sm = (LDSP char*)smem;
  if (b < 144) {
    const u32x4 xa = ld16(rw, tid * 16, WS_XT4 + b * 16384), xb = ld16(rw, tid * 16, WS_XT4 + b * 16384 + 8192);
    const u32x2 sa_ = ld8(rw, tid * 8, WS_ST1), sb_ = ld8(rw, tid * 8, WS_ST1 + 4096);
#pragma unroll
    for (int i = 0; i < 4; i++) dma16(rw, sm + C_KC + i * 8192 + w * 1024, lane * 16, WS_KC + b * 33280 + i * 8192 + w * 1024);
    if (tid < 32) dma16(rw, sm + C_KC + 32768, lane * 16, WS_KC + b * 33280 + 32768);
    float sh[4], scl[4], ga[4], dk[4];
#pragma unroll
    for (int c = 0; c < 4; c++) {
      const int d = 4 * b + c;
      sh[c] = rb(modp[d] + modp[3456 + d]);
      scl[c] = rb(1.f + rb(modp[576 + d] + modp[3456 + 576 + d]));
      ga[c] = rb(modp[1152 + d] + modp[3456 + 1152 + d]);
      dk[c] = b2f(dsk[d]);
    }
    if (tid < 128 && (tid & 31) < 30) *(LDSP u32x4*)(sm + C_HR + (tid >> 5) * C_HP + 4096 + (tid & 31) * 16) = (u32x4){0, 0, 0, 0};
    u32 hp[2][4];
#pragma unroll
    for (int hf = 0; hf < 2; hf++) {
      const u32x4 xv = hf ? xb : xa;
      const u32x2 sv = hf ? sb_ : sa_;
      const float m0 = hi2f(sv[0]), r0 = __builtin_amdgcn_rcpf(lo2f(sv[0])), m1 = hi2f(sv[1]), r1 = __builtin_amdgcn_rcpf(lo2f(sv[1]));
#pragma unroll
      for (int c = 0; c < 4; c++) {
        const float x0 = (c & 1) ? hi2f(xv[c >> 1]) : lo2f(xv[c >> 1]);
        const float x1 = (c & 1) ? hi2f(xv[2 + (c >> 1)]) : lo2f(xv[2 + (c >> 1)]);
        u32 t = pk2(x0 - m0, x1 - m1);
        t = pk2(lo2f(t) * r0, hi2f(t) * r1);
        t = pk2(lo2f(t) * scl[c], hi2f(t) * scl[c]);
        t = pk2(hi2f(t) + sh[c], lo2f(t) + sh[c]);
        hp[hf][c] = t;
        *(LDSP u32*)(sm + C_HR + c * C_HP + (2046 - 1024 * hf - 2 * tid) * 2) = t;
      }
    }
    WAIT_V(0);
    __syncthreads();
    {
      const int c = w >> 1, hw = w & 1, r16 = lane & 15, kg = lane >> 4;
      const int e = 8 * kg + r16 + 1;
      const LDSP char* pu = sm + C_KC + c * 8320 + (e & 1) * 4160 + (e >> 1) * 4 + hw * 256;
      const LDSP char* pv = sm + C_HR + c * C_HP + (240 - 16 * r16 + 8 * kg) * 2 + hw * 256;
      f32x4 acc[8];
#pragma unroll
      for (int i = 0; i < 8; i++) acc[i] = (f32x4){0, 0, 0, 0};
#pragma unroll
      for (int rho = 0; rho < 4; rho++) {
        bf16x8 U[8], V[8];
#pragma unroll
        for (int a = 0; a < 8; a++) {
          const LDSP u32* p = (const LDSP u32*)(pu + a * 512 + rho * 64);
          U[a] = asb((u32x4){p[0], p[1], p[2], p[3]});
          V[a] = *(const LDSP bf16x8*)(pv + (7 - a) * 512 + rho * 64);
        }
#pragma unroll
        for (int a = 0; a < 8; a++)
#pragma unroll
          for (int i = a; i < 8; i++) acc[i] = MFMA16(U[a], V[i - a], acc[i]);
      }
      LDSP char* py = sm + C_Y + hw * 32768 + c * 8192 + (16 * r16 + 4 * kg) * 4;
#pragma unroll
      for (int i = 0; i < 8; i++) *(LDSP f32x4*)(py + i * 1024) = acc[i];
    }
    __syncthreads();
#pragma unroll
    for (int hf = 0; hf < 2; hf++) {
      const u32x4 xv = hf ? xb : xa;
      float o0[4], o1[4];
#pragma unroll
      for (int c = 0; c < 4; c++) {
        const LDSP char* py = sm + C_Y + c * 8192 + hf * 4096 + tid * 8;
        const f32x2 ya = *(const LDSP f32x2*)py, yb = *(const LDSP f32x2*)(py + 32768);
        const u32 y = pk2(ya[0] + yb[0], ya[1] + yb[1]);
        const u32 hd = pk2(hi2f(hp[hf][c]) * dk[c], lo2f(hp[hf][c]) * dk[c]);
        u32 t = pk2(lo2f(y) + lo2f(hd), hi2f(y) + hi2f(hd));
        t = pk2(lo2f(t) * ga[c], hi2f(t) * ga[c]);
        const float x0 = (c & 1) ? hi2f(xv[c >> 1]) : lo2f(xv[c >> 1]);
        const float x1 = (c & 1) ? hi2f(xv[2 + (c >> 1)]) : lo2f(xv[2 + (c >> 1)]);
        o0[c] = x0 + lo2f(t);
        o1[c] = x1 + hi2f(t);
      }
      st16(rw, WS_X1T + b * 16384 + hf * 8192 + tid * 16, (u32x4){pk2(o0[0], o0[1]), pk2(o0[2], o0[3]), pk2(o1[0], o1[1]), pk2(o1[2], o1[3])});
    }
  } else if (b < 252) {
    const int pb = b - 144, S = pb / 3, part = pb - 3 * S;
    const int row = lane & 31, kg = lane >> 5;
    const u16* src = ((row >> 4) ? wu : wg) + 128 * S + 8 * (row & 15);
    const int ntask = w < 4 ? 2 : 1;
    u32x4 v[2][8];
#pragma unroll
    for (int tk = 0; tk < 2; tk++)
      if (tk < ntask) {
        const int ks = 12 * part + w + 8 * tk;
#pragma unroll
        for (int j = 0; j < 8; j++) v[tk][j] = *(const u32x4*)(src + (16 * ks + 8 * kg + j) * F_);
      }
    float cs[8], bi[8];
#pragma unroll
    for (int i = 0; i < 8; i++) cs[i] = bi[i] = 0.f;
#pragma unroll
    for (int tk = 0; tk < 2; tk++)
      if (tk < ntask) {
        const int ks = 12 * part + w + 8 * tk;
        const int k0 = 16 * ks + 8 * kg;
        float s[8], sb[8];
#pragma unroll
        for (int h = 0; h < 2; h++) {
          const f32x4 c0 = *(const f32x4*)(modp + 4 * 576 + k0 + 4 * h), c1 = *(const f32x4*)(modp + 3456 + 4 * 576 + k0 + 4 * h);
          const f32x4 d0 = *(const f32x4*)(modp + 3 * 576 + k0 + 4 * h), d1 = *(const f32x4*)(modp + 3456 + 3 * 576 + k0 + 4 * h);
#pragma unroll
          for (int j = 0; j < 4; j++) {
            s[4 * h + j] = rb(1.f + rb(c0[j] + c1[j]));
            sb[4 * h + j] = rb(d0[j] + d1[j]);
          }
        }
#pragma unroll
        for (int i = 0; i < 8; i++) {
          u32x4 o;
#pragma unroll
          for (int jp = 0; jp < 4; jp++) {
            const u32 r0 = v[tk][2 * jp][i >> 1], r1 = v[tk][2 * jp + 1][i >> 1];
            const float w0 = (i & 1) ? hi2f(r0) : lo2f(r0), w1 = (i & 1) ? hi2f(r1) : lo2f(r1);
            const u32 p = pk2(w0 * s[2 * jp], w1 * s[2 * jp + 1]);
            o[jp] = p;
            cs[i] = __builtin_amdgcn_fdot2_f32_bf16(__builtin_bit_cast(bf16x2, p), (bf16x2){(__bf16)1.f, (__bf16)1.f}, cs[i], false);
            bi[i] += w0 * sb[2 * jp] + w1 * sb[2 * jp + 1];
          }
          const int T = 8 * S + i, fb = T / 9, tin = T - 9 * fb;
          const int fidx = tin < 3 ? ks * 3 + tin : 108 + (tin - 3) * 36 + ks;
          st16(rw, WS_WP + (fb * 324 + fidx) * 1024 + lane * 16, o);
        }
      }
    LDSP float* red = (LDSP float*)sm;
#pragma unroll
    for (int i = 0; i < 8; i++) {
      red[(w * 16 + i) * 64 + lane] = cs[i];
      red[(w * 16 + 8 + i) * 64 + lane] = bi[i];
    }
    __syncthreads();
    {
      const int vv = tid >> 5, r = tid & 31;
      float s = 0.f;
#pragma unroll
      for (int i = 0; i < 8; i++) s += red[(i * 16 + vv) * 64 + r] + red[(i * 16 + vv) * 64 + 32 + r];
      st4(rw, WS_CSB + (((part * 288 + 8 * S + (vv & 7)) * 2 + (vv >> 3)) * 32 + r) * 4, __float_as_uint(s));
    }
  } else if (b == 252) {
    for (int i = tid; i < 3456; i += 512) st4(rw, WS_MODF + i * 4, __float_as_uint(rb(modp[i] + modp[3456 + i])));
  }
  STAMP_END(1);
}

constexpr int G_SLOT = 36864, G_CSB = 4 * G_SLOT;
#define DMA_SLOT(rs, slot, voffs, soff)                                                             \
  do {                                                                                              \
    _Pragma("unroll") for (int i_ = 0; i_ < 4; i_++) dma16(rs, sm + (slot)*G_SLOT + w * 4608 + i_ * 1024, voffs(i_), soff); \
    if (lane < 32) dma16(rs, sm + (slot)*G_SLOT + w * 4608 + 4096, voffs(4), soff);                \
  } while (0)

__global__ __launch_bounds__(512, 2) void k3(char* ws) {
  __shared__ __attribute__((aligned(16))) char smem[G_CSB + 2304];
  STAMP_BEGIN();
  const int b = blockIdx.x, tid = threadIdx.x, lane = tid & 63, w = __builtin_amdgcn_readfirstlane(tid >> 6);
  const int xcd = b & 7, idx = b >> 3;
  const int tt = (xcd & 1) * 4 + (idx & 3), fb = (xcd >> 1) * 8 + (idx >> 2);
  const rsrc_t rw = mkrs(ws, WS_END);
  LDSP char* sm = (LDSP char*)smem;
  const int hi = lane >> 5;

  float cb0[3], cb1[3];
#pragma unroll
  for (int p = 0; p < 3; p++) {
    cb0[p] = __uint_as_float(ld4(rw, tid * 4, WS_CSB + (p * 288 + fb * 9) * 256));
    cb1[p] = __uint_as_float(ld4(rw, (tid & 63) * 4 + 2048, WS_CSB + (p * 288 + fb * 9) * 256));
  }
  SCHED();
  const u32 wbase = WS_WP + fb * (324 * 1024) + w * 4608;
  const u32 wv = lane * 16;
#define VOFF3(i) (wv + (i)*1024)
  const u32 pv = (2 * hi + ((lane >> 4) & 1)) * 16384 + (lane & 15) * 16;
  const u32 pbase = WS_X1T + (tt * 256 + w * 32) * 8;
  u32x4 P[36];
#define LOADP(a, z) _Pragma("unroll") for (int ks = a; ks < z; ks++) P[ks] = ld16(rw, pv, pbase + ks * 65536)
  DMA_SLOT(rw, 0, VOFF3, wbase);
  SCHED();
  LOADP(0, 8);
  SCHED();
  DMA_SLOT(rw, 1, VOFF3, wbase + G_SLOT);
  SCHED();
  LOADP(8, 20);
  SCHED();
  DMA_SLOT(rw, 2, VOFF3, wbase + 2 * G_SLOT);
  SCHED();
  LOADP(20, 32);
  SCHED();
  DMA_SLOT(rw, 3, VOFF3, wbase + 3 * G_SLOT);
  SCHED();
  LOADP(32, 36);
  SCHED();
  {
    const float s0 = cb0[0] + cb0[1] + cb0[2], s1 = cb1[0] + cb1[1] + cb1[2];
    const int r = tid & 31, wh = (tid >> 5) & 1;
    const int pos = ((r >> 2) & 1) * 32 + wh * 16 + (r >> 4) * 8 + ((r >> 3) & 1) * 4 + (r & 3);
    *(LDSP float*)(sm + G_CSB + ((tid >> 6) * 64 + pos) * 4) = s0;
    if (tid < 64) *(LDSP float*)(sm + G_CSB + (8 * 64 + pos) * 4) = s1;
  }
  WAIT_V(51);
  WAIT_L(0);
  BAR();
  SCHED();

  f32x16 acc[3];
#pragma unroll
  for (int t = 0; t < 3; t++)
#pragma unroll
    for (int r = 0; r < 16; r++) acc[t][r] = 0.f;
  const LDSP char* fr = sm + lane * 16;
  constexpr int PD = 4;
  bf16x8 A[PD];
#define RDF(u) (*(const LDSP bf16x8*)(fr + (((u) / 36) & 3) * G_SLOT + ((u) % 36) * 1024))
#pragma unroll
  for (int u = 0; u < PD; u++) A[u] = RDF(u);
  float sum = 0.f, sq = 0.f, rs = 0.f, nrm = 0.f;
  const u32 hbase = WS_HT + ((tt * 8 + w) * 288 + fb * 9) * 1024;
  auto epi = [&](int tile, const f32x16& a) {
    const LDSP f32x4* cp = (const LDSP f32x4*)(sm + G_CSB + (tile * 64 + hi * 32) * 4);
    u32x4 o;
#pragma unroll
    for (int h = 0; h < 2; h++) {
      const f32x4 cg = cp[h], cu = cp[2 + h], bg = cp[4 + h], bu = cp[6 + h];
      float hv[4];
#pragma unroll
      for (int j = 0; j < 4; j++) {
        const float g = rs * a[4 * h + j] + (nrm * cg[j] + bg[j]);
        const float uu = rs * a[8 + 4 * h + j] + (nrm * cu[j] + bu[j]);
        hv[j] = g * uu;
      }
      o[2 * h] = pk2(hv[0], hv[1]);
      o[2 * h + 1] = pk2(hv[2], hv[3]);
    }
    __builtin_amdgcn_raw_buffer_store_b128(o, rw, wv, hbase + tile * 1024, 16);
  };
#pragma unroll
  for (int ks = 0; ks < 36; ks++) {
    if (ks % 12 == 8) {
      SCHED();
      if (ks == 8) WAIT_V(38);
      if (ks == 20) WAIT_V(21);
      if (ks == 32) WAIT_V(9);
      BAR();
      if (ks == 20) DMA_SLOT(rw, 0, VOFF3, wbase + 4 * G_SLOT);
      if (ks == 32) DMA_SLOT(rw, 1, VOFF3, wbase + 5 * G_SLOT);
      SCHED();
    }
    {
      auto s0 = __builtin_amdgcn_permlane16_swap(P[ks][0], P[ks][2], false, false);
      auto s1 = __builtin_amdgcn_permlane16_swap(P[ks][1], P[ks][3], false, false);
      P[ks] = (u32x4){s0[0], s1[0], s0[1], s1[1]};
    }
#pragma unroll
    for (int t = 0; t < 3; t++) {
      const int u = ks * 3 + t;
      const bf16x8 a = A[u % PD];
      A[u % PD] = RDF(u + PD);
      acc[t] = MFMA32(a, asb(P[ks]), acc[t]);
    }
#pragma unroll
    for (int j = 0; j < 4; j++) {
      const float x0 = lo2f(P[ks][j]), x1 = hi2f(P[ks][j]);
      sum += x0;
      sum += x1;
      sq = fmaf(x0, x0, sq);
      sq = fmaf(x1, x1, sq);
    }
    asm volatile("" : "+v"(sum), "+v"(sq));
    SCHED();
  }
  {
    sum += __shfl_xor(sum, 32);
    sq += __shfl_xor(sq, 32);
    const float mean = sum * (1.f / D_);
    const float var = fmaxf(sq * (1.f / D_) - mean * mean, 0.f);
    const float sd = rb(sqrtf(rb(rb(var) + 1e-5f)));
    rs = 1.f / sd;
    nrm = -rs * rb(mean);
  }
  epi(0, acc[0]);
#pragma unroll
  for (int s = 3; s < 9; s++) {
    f32x16 c;
#pragma unroll
    for (int r = 0; r < 16; r++) c[r] = 0.f;
#pragma unroll
    for (int ks = 0; ks < 36; ks++) {
      const int u = s * 36 + ks;
      if (ks == 24 && s < 8) {
        SCHED();
        if (s == 3) WAIT_V(8);
        if (s == 4) WAIT_V(9);
        if (s == 5) WAIT_V(7);
        if (s == 6) WAIT_V(7);
        if (s == 7) WAIT_V(2);
        BAR();
        if (s + 3 < 9) DMA_SLOT(rw, (s - 1) & 3, VOFF3, wbase + (s + 3) * G_SLOT);
        SCHED();
      }
      const bf16x8 a = A[u % PD];
      if (u + PD < 324) A[u % PD] = RDF(u + PD);
      c = MFMA32(a, asb(P[ks]), c);
      if (s == 3 && ks == 2) epi(1, acc[1]);
      if (s == 3 && ks == 12) epi(2, acc[2]);
      if (s > 3 && ks == 4) epi(s - 1, acc[0]);
    }
    acc[0] = c;
  }
  epi(8, acc[0]);
  STAMP_END(2);
}

__global__ __launch_bounds__(512, 2) void k4(const u16* __restrict__ wd, char* ws, u16* __restrict__ out) {
  __shared__ __attribute__((aligned(16))) char smem[4 * G_SLOT];
  STAMP_BEGIN();
  const int b = blockIdx.x, tid = threadIdx.x, lane = tid & 63, w = __builtin_amdgcn_readfirstlane(tid >> 6);
  const int xcd = b & 7, idx = b >> 3;
  const int j = xcd >> 1, tb = (xcd & 1) * 8 + (idx & 7), db = idx >> 3;
  const int tg = w & 3, kh = w >> 2;
  const rsrc_t rw = mkrs(ws, WS_END);
  const rsrc_t rd = mkrs(wd, F_ * D_ * 2);
  LDSP char* sm = (LDSP char*)smem;
  const int c16 = lane & 15, kg = lane >> 4;

  u32 dv[5];
#pragma unroll
  for (int i = 0; i < 5; i++) {
    const int p = w * 288 + i * 64 + lane;
    const int rho = p / 18, cc = p - 18 * rho;
    const int f = 8 * (rho & 15) + (rho >> 4);
    dv[i] = f * 1152 + (cc < 16 ? db * 256 + cc * 16 : 1024 + db * 32 + (cc - 16) * 16);
  }
#define VOFF4(i) dv[i]
  const u32 dbase = j * 1152 * 1152;
  const u32 hv = kg * 512 + c16 * 16;
  const u32 hb = WS_HT + ((tb * 4 + tg) * 288 + 72 * j + 4 * kh) * 1024;
  u32x4 H[9][4];
#define LOADH(s)                                                                      \
  _Pragma("unroll") for (int q_ = 0; q_ < 4; q_++) H[s][q_] = ld16(rw, hv + (q_ >> 1) * 2048 + (q_ & 1) * 256, hb + (s)*8192)
#pragma unroll
  for (int s = 0; s < 4; s++) {
    DMA_SLOT(rd, s, VOFF4, dbase + s * 128 * 1152);
    SCHED();
    LOADH(s);
    SCHED();
  }
  f32x4 acc[2][9];
#pragma unroll
  for (int t = 0; t < 9; t++) acc[0][t] = acc[1][t] = (f32x4){0, 0, 0, 0};
  const LDSP char* tr = sm + (16 * (4 * kh + (kg >> 1)) + 4 * (kg & 1) + (c16 >> 2)) * 288 + (c16 & 3) * 8;
  auto rdA = [&](int u) -> bf16x8 {
    const int s = u / 18, r = u % 18, ksl = r / 9, t = r % 9;
    const LDSP char* p = tr + (s & 3) * G_SLOT + ksl * (32 * 288) + t * 32;
    const s16x4 lo = __builtin_amdgcn_ds_read_tr16_b64_v4i16((LDSP s16x4*)p);
    const s16x4 hi = __builtin_amdgcn_ds_read_tr16_b64_v4i16((LDSP s16x4*)(p + 8 * 288));
    const u32x2 a = __builtin_bit_cast(u32x2, lo), c = __builtin_bit_cast(u32x2, hi);
    return asb((u32x4){a[0], a[1], c[0], c[1]});
  };
  WAIT_V(31);
  BAR();
  SCHED();
  constexpr int PD = 3;
  bf16x8 A[PD];
#pragma unroll
  for (int u = 0; u < PD; u++) A[u] = rdA(u);
#pragma unroll
  for (int s = 0; s < 9; s++) {
#pragma unroll
    for (int r = 0; r < 18; r++) {
      const int u = s * 18 + r;
      if (r == 12 && s < 8) {
        SCHED();
        if (s == 0) WAIT_V(22);
        if (s >= 1 && s <= 6) WAIT_V(13);
        if (s == 7) WAIT_V(4);
        BAR();
        if (s >= 1 && s + 3 < 9) {
          DMA_SLOT(rd, (s - 1) & 3, VOFF4, dbase + (s + 3) * 128 * 1152);
          SCHED();
          LOADH(s + 3);
        }
        SCHED();
      }
      const bf16x8 a = A[u % PD];
      if (u + PD < 162) A[u % PD] = rdA(u + PD);
      const int ksl = r / 9, t = r % 9;
      acc[0][t] = MFMA16(a, asb(H[s][2 * ksl]), acc[0][t]);
      acc[1][t] = MFMA16(a, asb(H[s][2 * ksl + 1]), acc[1][t]);
    }
  }
  __syncthreads();
  {
    LDSP char* pw = sm + (w ^ 4) * 9216 + lane * 16;
#pragma unroll
    for (int t = 0; t < 9; t++) *(LDSP f32x4*)(pw + t * 1024) = kh ? acc[0][t] : acc[1][t];
  }
  __syncthreads();
  f32x4 r[9];
  {
    const LDSP char* pr = sm + w * 9216 + lane * 16;
#pragma unroll
    for (int t = 0; t < 9; t++) r[t] = (kh ? acc[1][t] : acc[0][t]) + *(const LDSP f32x4*)(pr + t * 1024);
  }
  const int grp = (idx << 3) | (xcd & 1);
  const int tok = tb * 128 + tg * 32 + 2 * c16 + kh;
  u32x2 x1v[9];
  f32x4 gbv[9];
  if (tg != j) {
    const u32 xo = WS_XCH + (((grp + 2 * tg) * 4 + j) * 2 + kh) * 4608;
#pragma unroll
    for (int t = 0; t < 8; t += 2)
      st16(rw, xo + t * 512 + lane * 16, (u32x4){pk2(r[t][0], r[t][1]), pk2(r[t][2], r[t][3]), pk2(r[t + 1][0], r[t + 1][1]), pk2(r[t + 1][2], r[t + 1][3])});
    st8(rw, xo + 4096 + lane * 8, (u32x2){pk2(r[8][0], r[8][1]), pk2(r[8][2], r[8][3])});
    WAIT_V(0);
  } else {
#pragma unroll
    for (int t = 0; t < 9; t++) {
      const int d = (t < 8 ? 128 * db + 16 * t : 512 + 16 * db) + 4 * kg;
      x1v[t] = ld8(rw, (d >> 2) * 16384 + tok * 8, WS_X1T);
      gbv[t] = __builtin_bit_cast(f32x4, ld16(rw, d * 4, WS_MODF + 5 * 576 * 4));
    }
  }
  BAR();
  if (tid == 0) st4(rw, WS_FLAG + b * 128, 1u);
  if (tg == j) {
    const int src = (lane & 3);
    const u32 fo = WS_FLAG + (grp + 2 * src) * 128;
    for (int spin = 0; spin < (1 << 18); spin++) {
      const u32 f = __hip_atomic_load((const u32*)(ws + fo), __ATOMIC_RELAXED, __HIP_MEMORY_SCOPE_AGENT);
      if (__all(f != 0u || src == j)) break;
      __builtin_amdgcn_s_sleep(1);
      asm volatile("" ::: "memory");
    }
    asm volatile("" ::: "memory");
#pragma unroll
    for (int sI = 1; sI < 4; sI++) {
      const int sj = (j + sI) & 3;
      const u32 xo = WS_XCH + ((b * 4 + sj) * 2 + kh) * 4608;
      u32x4 v[4];
#pragma unroll
      for (int q = 0; q < 4; q++) v[q] = __builtin_amdgcn_raw_buffer_load_b128(rw, lane * 16, xo + q * 1024, 16);
      const u32x2 v8 = __builtin_amdgcn_raw_buffer_load_b64(rw, lane * 8, xo + 4096, 16);
#pragma unroll
      for (int q = 0; q < 4; q++) {
        r[2 * q] += (f32x4){lo2f(v[q][0]), hi2f(v[q][0]), lo2f(v[q][1]), hi2f(v[q][1])};
        r[2 * q + 1] += (f32x4){lo2f(v[q][2]), hi2f(v[q][2]), lo2f(v[q][3]), hi2f(v[q][3])};
      }
      r[8] += (f32x4){lo2f(v8[0]), hi2f(v8[0]), lo2f(v8[1]), hi2f(v8[1])};
    }
#pragma unroll
    for (int t = 0; t < 9; t++) {
      const int d = (t < 8 ? 128 * db + 16 * t : 512 + 16 * db) + 4 * kg;
      float o[4];
#pragma unroll
      for (int e = 0; e < 4; e++) {
        const float xv = (e & 1) ? hi2f(x1v[t][e >> 1]) : lo2f(x1v[t][e >> 1]);
        o[e] = xv + rb(gbv[t][e] * rb(r[t][e]));
      }
      *(u32x2*)(out + tok * D_ + d) = (u32x2){pk2(o[0], o[1]), pk2(o[2], o[3])};
    }
  }
  STAMP_END(3);
}

extern "C" void kernel_launch(void* const* d_in, const int* in_sizes, int n_in, void* d_out, int out_size, void* d_ws, size_t ws_size,
                              hipStream_t stream) {
  const u16* const* in = (const u16* const*)d_in;
  char* ws = (char*)d_ws;
  k1<<<256, 512, 0, stream>>>(in[0], in[1], in[2], in[3], in[4], in[5], in[6], in[7], in[13], in[14], ws);
  k2<<<256, 512, 0, stream>>>(in[8], in[10], in[11], ws);
  k3<<<256, 512, 0, stream>>>(ws);
  k4<<<256, 512, 0, stream>>>(in[12], ws, (u16*)d_out);
}
```

```cpp
#include <hip/hip_runtime.h>
#include <stdint.h>

typedef unsigned short u16;
typedef uint32_t u32;
typedef __attribute__((ext_vector_type(8))) __bf16 bf16x8;
typedef __attribute__((ext_vector_type(2))) __bf16 bf16x2;
typedef __attribute__((ext_vector_type(4))) short s16x4;
typedef __attribute__((ext_vector_type(2))) float f32x2;
typedef __attribute__((ext_vector_type(4))) float f32x4;
typedef __attribute__((ext_vector_type(16))) float f32x16;
typedef __attribute__((ext_vector_type(2))) u32 u32x2;
typedef __attribute__((ext_vector_type(4))) u32 u32x4;
typedef __attribute__((ext_vector_type(8))) u32 u32x8;
typedef __attribute__((ext_vector_type(16))) u32 u32x16;
typedef __amdgpu_buffer_rsrc_t rsrc_t;
#define LDSP __attribute__((address_space(3)))
#define DEV __device__ __forceinline__

constexpr int L_ = 2048, D_ = 576, F_ = 4608;

constexpr u32 WS_ST2 = 0;
constexpr u32 WS_FLG = 16384;
constexpr u32 WS_EP = 20480;
constexpr u32 WS_ARR = 24576;
constexpr u32 WS_GS = 32768;
constexpr u32 WS_GM = 393216;
constexpr u32 WS_MODF = 65536;
constexpr u32 WS_CSB = 98304;
constexpr u32 WS_X1T = 1u << 20;
constexpr u32 WS_X1S = WS_X1T + 2359296;
constexpr u32 WS_WP = WS_X1S + 2359296;
constexpr u32 WS_WD = WS_WP + 32 * 324 * 1024;
constexpr u32 WS_HT = WS_WD + 16 * 324 * 1024;
constexpr u32 WS_XCH = WS_HT + 64 * 288 * 1024;
constexpr u32 WS_END = WS_XCH + 256 * 32768;

DEV float lo2f(u32 v) { return __uint_as_float(v << 16); }
DEV float hi2f(u32 v) { return __uint_as_float(v & 0xffff0000u); }
DEV float b2f(u16 v) { return __uint_as_float(((u32)v) << 16); }
DEV u32 pk2(float a, float b) { bf16x2 r = {(__bf16)a, (__bf16)b}; return __builtin_bit_cast(u32, r); }
DEV float rb(float a) { return (float)(__bf16)a; }
DEV rsrc_t mkrs(const void* p, u32 bytes) { return __builtin_amdgcn_make_buffer_rsrc((void*)p, 0, (int)bytes, 0x00020000); }
DEV void st16(rsrc_t rs, u32 off, u32x4 v) { __builtin_amdgcn_raw_buffer_store_b128(v, rs, off, 0, 16); }
#define BAUX 16
DEV void st16b(rsrc_t rs, u32 off, u32x4 v) { __builtin_amdgcn_raw_buffer_store_b128(v, rs, off, 0, BAUX); }
#define LA 2
DEV u32x4 ld16n(rsrc_t rs, u32 voff, u32 soff) { return __builtin_amdgcn_raw_buffer_load_b128(rs, voff, soff, LA); }
#define XS 0
DEV void st8(rsrc_t rs, u32 off, u32x2 v) { __builtin_amdgcn_raw_buffer_store_b64(v, rs, off, 0, 16); }
DEV void st4(rsrc_t rs, u32 off, u32 v) { __builtin_amdgcn_raw_buffer_store_b32(v, rs, off, 0, 16); }
DEV u32x4 ld16(rsrc_t rs, u32 voff, u32 soff) { return __builtin_amdgcn_raw_buffer_load_b128(rs, voff, soff, 0); }
DEV u32x2 ld8(rsrc_t rs, u32 voff, u32 soff) { return __builtin_amdgcn_raw_buffer_load_b64(rs, voff, soff, 0); }
DEV u32 ld4(rsrc_t rs, u32 voff, u32 soff) { return __builtin_amdgcn_raw_buffer_load_b32(rs, voff, soff, 0); }
DEV void dma16(rsrc_t rs, LDSP char* dst, u32 voff, u32 soff) {
  __builtin_amdgcn_raw_ptr_buffer_load_lds(rs, (LDSP void*)dst, 16, voff, soff, 0, 0);
}
#define WAIT_V(n) asm volatile("s_waitcnt vmcnt(" #n ")" ::: "memory")
#define WAIT_L(n) asm volatile("s_waitcnt lgkmcnt(" #n ")" ::: "memory")
#define BAR() do { __builtin_amdgcn_s_barrier(); asm volatile("" ::: "memory"); } while (0)
#define SCHED() __builtin_amdgcn_sched_barrier(0)
#define MFMA32(a, b, c) __builtin_amdgcn_mfma_f32_32x32x16_bf16(a, b, c, 0, 0, 0)
#define MFMA16(a, b, c) __builtin_amdgcn_mfma_f32_16x16x32_bf16(a, b, c, 0, 0, 0)
template <int I> struct IC { static constexpr int v = I; };
template <int B, int E, class F> DEV void sfor(F&& f) {
  if constexpr (B < E) { f(IC<B>{}); sfor<B + 1, E>(f); }
}
template <int N> DEV void wait_v() { asm volatile("s_waitcnt vmcnt(%0)" ::"n"(N) : "memory"); }
DEV bf16x8 asb(u32x4 v) { return __builtin_bit_cast(bf16x8, v); }

DEV u32x4 tcol(const u32x4* v, int i) {
  const u32 sel = (i & 1) ? 0x07060302u : 0x05040100u;
  u32x4 o;
#pragma unroll
  for (int jp = 0; jp < 4; jp++) o[jp] = __builtin_amdgcn_perm(v[2 * jp + 1][i >> 1], v[2 * jp][i >> 1], sel);
  return o;
}

#define STAMP_BEGIN()
#define STAMPI(i)
#define STAMP_END(k)
#define STAMP_END3(k)

DEV float dadd(float v, int ror) {
  const int t = __float_as_int(v);
  return v + __int_as_float(ror == 1 ? __builtin_amdgcn_mov_dpp(t, 0x121, 15, 15, true) : ror == 2 ? __builtin_amdgcn_mov_dpp(t, 0x122, 15, 15, true)
                          : ror == 4 ? __builtin_amdgcn_mov_dpp(t, 0x124, 15, 15, true) : __builtin_amdgcn_mov_dpp(t, 0x128, 15, 15, true));
}
DEV float sw16(float a, float b) {
  const auto r = __builtin_amdgcn_permlane16_swap(__float_as_uint(a), __float_as_uint(b), false, false);
  return __uint_as_float(r[0]) + __uint_as_float(r[1]);
}
DEV float sw32(float a, float b) {
  const auto r = __builtin_amdgcn_permlane32_swap(__float_as_uint(a), __float_as_uint(b), false, false);
  return __uint_as_float(r[0]) + __uint_as_float(r[1]);
}
DEV float hsum(float s) {
  s = dadd(dadd(dadd(dadd(s, 8), 4), 2), 1);
  return sw16(s, s);
}
DEV u32x4 ld16c(rsrc_t rs, u32 voff, u32 soff) { return __builtin_amdgcn_raw_buffer_load_b128(rs, voff, soff, 16); }
DEV void ln_load(rsrc_t rx, int task, int lane, u32x4* v) {
  const u32 vo = (lane & 31) < 24 ? (lane >> 5) * 1152 + (lane & 31) * 16 : 0x80000000u;
#pragma unroll
  for (int i = 0; i < 3; i++) v[i] = __builtin_amdgcn_raw_buffer_load_b128(rx, vo + 384 * i, task * 2304, XS);
}
DEV void ln_stat(rsrc_t rw, int task, int lane, const u32x4* v, u32 tag) {
  f32x2 s2 = {0.f, 0.f};
#pragma unroll
  for (int i = 0; i < 12; i++) s2 += (f32x2){lo2f(v[i >> 2][i & 3]), hi2f(v[i >> 2][i & 3])};
  const float mean = hsum(s2[0] + s2[1]) * (1.f / D_);
  f32x2 q2 = {0.f, 0.f};
#pragma unroll
  for (int i = 0; i < 12; i++) {
    const f32x2 e = (f32x2){lo2f(v[i >> 2][i & 3]), hi2f(v[i >> 2][i & 3])} - mean;
    q2 += e * e;
  }
  const float q = hsum((lane & 31) < 24 ? q2[0] + q2[1] : 0.f);
  const float sd = __builtin_amdgcn_sqrtf(rb(rb(q * (1.f / D_)) + 1e-5f));
  if ((lane & 31) == 0) st8(rw, WS_GS + task * 16 + (lane >> 5) * 8, (u32x2){pk2(sd, mean), tag});
}
constexpr int KG_PITCH = 1040, KG_CP = 53 * KG_PITCH;
constexpr int KG_TAB = 2 * KG_CP;
constexpr int C_KC = 0, C_Y = 66560, C_HR = C_Y + 65536, C_HP = 4608;
DEV f32x2 cm(f32x2 a, f32x2 b) {
  f32x2 t, d;
  asm("v_pk_mul_f32 %0, %1, %2 op_sel:[0,0] op_sel_hi:[0,1]" : "=v"(t) : "v"(a), "v"(b));
  asm("v_pk_fma_f32 %0, %1, %2, %3 op_sel:[1,1,0] op_sel_hi:[1,0,1] neg_lo:[0,1,0]" : "=v"(d) : "v"(a), "v"(b), "v"(t));
  return d;
}
DEV f32x2 cexpm(float zr2, float zrev, float m) {
  const float e = __builtin_amdgcn_exp2f(zr2 * m);
  const float f = __builtin_amdgcn_fractf(zrev * m);
  return (f32x2){e * __builtin_amdgcn_cosf(f), e * __builtin_amdgcn_sinf(f)};
}
template <int ROW> DEV void put(u32 v) { asm volatile("ds_write_addtid_b32 %0 offset:%1" ::"v"(v), "n"(ROW * KG_PITCH) : "memory"); }
#define PIN(x) asm volatile("" : "+v"(x))
#define DLY 0
#define WDP 3
#define EB 0
#define EBP 1
#define PF 0
#define GATE 0
#define XAUX 0
DEV void wd_addr1(u32 task, u32 lane, u32& so, u32& dst) {
  const u32 Ll = task * 64 + lane, ks = Ll / 288, rem = Ll - 288 * ks;
  const u32 j = ks / 36, r36 = ks - 36 * j, s = r36 >> 2, kh = (r36 >> 1) & 1, ksl = r36 & 1;
  const u32 cp = rem >> 3, hh = (rem >> 2) & 1, kg = rem & 3;
  so = ((1152 * j + 128 * s + 32 * kg + 4 * kh + 2 * ksl) * D_ + 16 * cp + 8 * hh) * 2;
  const u32 db = cp / 9, Tl = cp - 9 * db, q = 2 * s + ksl, a = ((Tl >> 1) - j - 1) & 3;
  const u32 keep = (Tl & 1) == kh, t8 = Tl >> 3, a3 = (a + 1) >> 2 & ~t8;
  const u32 v = q * (7 - 6 * a3) + (1 - t8) * (1 + 3 * keep + a + a3 * (122 + 15 * keep));
  dst = WS_WD + ((j * 4 + db) * 324 + 2 * v + kh) * 1024 + hh * 64 + kg * 16;
}
DEV void wd_addr(u32 task, u32 lane, u32& so, u32& dst) {
  u32 t;
  wd_addr1(task, ((lane >> 1) & 7) * 8 + (lane & 1) * 4 + (lane >> 4), so, t);
  wd_addr1(task, lane, t, dst);
}
DEV void wd_load(const u16* wd, u32 so, u32x4* dv) {
  const rsrc_t rd = mkrs(wd, D_ * F_ * 2);
#pragma unroll
  for (int e = 0; e < 8; e++) dv[e] = ld16n(rd, so + (e >> 2) * D_ * 2, 8 * (e & 3) * D_ * 2);
}
DEV void wd_store(rsrc_t rw, u32 dst, const u32x4* dv, int lane) {
  const int src = (lane & 3) * 64 + (lane >> 3) * 8 + (lane & 4);
#pragma unroll
  for (int i = 0; i < 8; i++) {
    u32x4 t = tcol(dv, i);
#pragma unroll
    for (int j = 0; j < 4; j++) t[j] = __builtin_amdgcn_ds_bpermute(src, t[j]);
    st16b(rw, (dst ^ ((i & 2) * 16)) + i * 128, t);
  }
}
constexpr int SPIN_MAX = 1 << 16;
#define LBAR() do { WAIT_L(0); BAR(); } while (0)
#define CFENCE() asm volatile("" ::: "memory")
__global__ __launch_bounds__(512, 2) void k1(const u16* __restrict__ x, const u16* __restrict__ Are, const u16* __restrict__ Aim,
                                             const u16* __restrict__ Bre, const u16* __restrict__ Bim, const u16* __restrict__ Cre,
                                             const u16* __restrict__ Cim, const u16* __restrict__ ldt, const u16* __restrict__ dsk,
                                             const u16* __restrict__ cvec, const u16* __restrict__ wmod, const u16* __restrict__ wg,
                                             const u16* __restrict__ wu, const u16* __restrict__ wd, char* ws) {
  __shared__ __attribute__((aligned(16))) char smem[C_HR + 4 * C_HP];
  STAMP_BEGIN();
  const int b = blockIdx.x, tid = threadIdx.x, lane = tid & 63, w = __builtin_amdgcn_readfirstlane(tid >> 6);
  const rsrc_t rw = mkrs(ws, WS_END), rx = mkrs(x, L_ * D_ * 2);
  LDSP char* sm = (LDSP char*)smem;
  asm volatile("" ::"s"(Are), "s"(Aim), "s"(Bre), "s"(Bim), "s"(Cre), "s"(Cim), "s"(ldt), "s"(dsk), "s"(cvec), "s"(wmod), "s"(wg), "s"(wu), "s"(wd));
#define MKTAG() asm volatile("v_add_u32 %0, 0x9e3779b9, %0" : "+v"(tag))
  if (b < 144) {
    const int cg = (b & 7) * 18 + (b >> 3);
    const int ch = 4 * cg + (w >> 1), n = (w & 1) * 64 + lane;
    const int pi = ch * 128 + n;
    u16 pr_[7] = {Are[pi], Aim[pi], Bre[pi], Bim[pi], Cre[pi], Cim[pi], ldt[ch]};
    const u32x2 dkv = *(const u32x2*)(dsk + 4 * cg);
    u32 tag = __builtin_amdgcn_raw_buffer_load_b32(rw, 0, WS_EP, 16);
    SCHED();
    BAR();
    u32x2 xr[4];
#pragma unroll
    for (int i = 0; i < 2; i++) xr[i] = __builtin_amdgcn_raw_buffer_load_b64(rx, tid * 2304 + i * 1152, cg * 8, XAUX);
    SCHED();
    if (tid < 128 && (tid & 31) < 30) *(LDSP u32x4*)(sm + C_HR + (tid >> 5) * C_HP + 4096 + (tid & 31) * 16) = (u32x4){0, 0, 0, 0};
    const int r16 = lane & 15, q = lane >> 4, ct = w & 1;
    LDSP char* tabb = sm + KG_TAB + (w >> 1) * 4112;
    LDSP char* pa = sm + (w >> 2) * KG_CP + (w & 2) * 256 + r16 * KG_PITCH + q * 16;
    LDSP char* ts0 = tabb + 256 * ct + 16 * r16;
    LDSP char* ts1 = ts0 + (q == 0 ? 0 : q == 1 ? 8 : q == 2 ? 512 : 1024);
    LDSP char* ts2 = ts1 + (q < 2 ? 2 : q == 2 ? 4 : 8);
    LDSP char* ts3 = ts1 + (q < 2 ? 4 : q == 2 ? 8 : 512);
    LDSP char* ts4 = ts3 + (q < 2 ? 2 : q == 2 ? 4 : 8);
    LDSP char* te[11];
#pragma unroll
    for (int j = 0; j < 11; j++) te[j] = (j & 3) ? tabb + 2 * (int)rb((float)(8 * n + j)) : tabb + 16 * n + 2 * j;
    const int ee = 8 * q + r16 + 1;
    u32 pu = (u32)(uintptr_t)(sm + C_KC + (w >> 1) * 16640 + (ee & 3) * 4160 + (ee >> 2) * 8 + ct * 256);
    u32 pv = (u32)(uintptr_t)(sm + C_HR + (w >> 1) * C_HP + (240 - 16 * r16 + 8 * q) * 2 + ct * 256);
    u32 py = (u32)(uintptr_t)(sm + C_Y + ct * 32768 + (w >> 1) * 8192 + 64 * r16 + 16 * (q ^ ((r16 >> 1) & 3)));
    u32 dso, dst;
    wd_addr(4 * b + (w & 3), lane, dso, dst);
    u32 gmo = lane < 16 ? (lane >> 3) * 27648 + (((lane >> 1) & 3) + ((lane & 6) == 6)) * 4608 + (lane & 1) * 16 : 0;
    PIN(dso); PIN(dst); PIN(gmo); PIN(pa); PIN(ts0); PIN(ts1); PIN(ts2); PIN(ts3); PIN(ts4); PIN(pu); PIN(pv); PIN(py);
#pragma unroll
    for (int j = 0; j < 11; j++) if (j == 0 || (j & 3)) PIN(te[j]);
    asm volatile("s_mov_b32 m0, %0" ::"s"((u32)(uintptr_t)sm + (w >> 2) * KG_CP + (w & 3) * 256));
    SCHED();
#pragma unroll
    for (int i = 0; i < 7; i++) PIN(pr_[i]);
    const float are = b2f(pr_[0]), aim = b2f(pr_[1]), bre = b2f(pr_[2]), bim = b2f(pr_[3]), cre = b2f(pr_[4]), cim = b2f(pr_[5]);
    const float dt = rb(__expf(b2f(pr_[6])));
    const u32x2 dkp = {(u32)__builtin_amdgcn_readfirstlane(dkv[0]), (u32)__builtin_amdgcn_readfirstlane(dkv[1])};
    STAMPI(1);
    const float zr2 = dt * are * 1.44269504088896341f, zrev = dt * aim * 0.15915494309189535f;
    {
      const f32x2 w8 = cexpm(zr2, zrev, 8.f), w1 = cexpm(zr2, -zrev, 1.f), w256 = cexpm(zr2, -zrev, 256.f);
      const f32x2 w16 = cm(w8, w8);
      f32x2 q0 = w16, q1 = w8, p[21];
      p[0] = cm((f32x2){cre, -cim}, (f32x2){dt * bre, -dt * bim});
      put<21>(0x3f80u);
      sfor<0, 21>([&](auto I) {
        constexpr int a = I.v;
        constexpr int src[21] = {0, 0, 1, 2, 3, 4, 5, 6, 0, 2, 4, 6, 8, 10, 12, 13, 14, 16, 17, 18, 19};
        if constexpr (a > 0) p[a] = cm(p[src[a]], a < 8 ? w1 : w256);
        if constexpr (a < 16) {
          put<22 + 2 * a>(pk2(q1[0], q1[1]));
          if constexpr (a < 15) { q1 = cm(q1, w16); put<23 + 2 * a>(pk2(q0[0], q0[1])); }
          if constexpr (a < 14) q0 = cm(q0, w16);
        }
        put<a>(pk2(p[a][0], p[a][1]));
      });
    }
#pragma unroll
    for (int i = 0; i < 2; i++) xr[2 + i] = __builtin_amdgcn_raw_buffer_load_b64(rx, tid * 2304 + i * 1152, cg * 8 + 1024 * 1152, XAUX);
    u32x4 dv[8];
    SCHED();
    if (WDP == 0 && w < 4) wd_load(wd, dso, dv);
    SCHED();
    STAMPI(2);
    MKTAG();
    LBAR();
    if (WDP == 1 && w < 4) wd_load(wd, dso, dv);
    SCHED();
    STAMPI(3);
    {
      f32x4 a0 = {0, 0, 0, 0}, a1 = {0, 0, 0, 0};
      bf16x8 fb[8], f0[8], f1[8];
#pragma unroll
      for (int ks = 0; ks < 8; ks++) {
        fb[ks] = *(LDSP bf16x8*)(pa + (21 + 16 * ct) * KG_PITCH + ks * 64);
        f0[ks] = *(LDSP bf16x8*)(pa + ks * 64);
        f1[ks] = *(LDSP bf16x8*)(pa + 16 * KG_PITCH + ks * 64);
      }
      SCHED();
      if (tid == 0) st4(rw, WS_ARR + b * 4, tag);
#pragma unroll
      for (int ks = 0; ks < 8; ks++) {
        a0 = MFMA16(f0[ks], fb[ks], a0);
        a1 = MFMA16(f1[ks], fb[ks], a1);
      }
      const u32 p01 = pk2(a0[0], a0[1]), p23 = pk2(a0[2], a0[3]), s01 = pk2(a1[0], a1[1]), s23 = pk2(a1[2], a1[3]);
      *(LDSP u16*)ts1 = (u16)p01;
      *(LDSP u16*)ts2 = (u16)(p01 >> 16);
      *(LDSP u16*)ts3 = (u16)p23;
      *(LDSP u16*)ts4 = (u16)(p23 >> 16);
      if (q == 0) {
        *(LDSP u16*)(ts0 + 2048) = (u16)s01;
        *(LDSP u16*)(ts0 + 2560) = (u16)(s01 >> 16);
        *(LDSP u16*)(ts0 + 3072) = (u16)s23;
        *(LDSP u16*)(ts0 + 3584) = (u16)(s23 >> 16);
      } else if (n == 16) {
        *(LDSP u16*)(ts0 + 4096) = (u16)s01;
      }
    }
    u32x4 gs[2], gmr;
    auto gload = [&]() {
      gs[0] = ld16c(rw, tid * 16, WS_GS);
      gs[1] = ld16c(rw, tid * 16, WS_GS + 8192);
      gmr = ld16c(rw, gmo, WS_GM + cg * 32);
    };
    gload();
    STAMPI(4);
    LBAR();
    STAMPI(5);
    {
      LDSP char* ob = sm + C_KC + (w >> 1) * 16640 + 32 + 16 * n;
      u32 e[11], ev[6], od[5];
#pragma unroll
      for (int j = 0; j < 11; j++) e[j] = *(const LDSP u16*)((j & 3) ? te[j] : te[0] + 2 * j);
      const u32 ea = *(const LDSP u16*)(te[0] + 2048), eb = *(const LDSP u16*)(te[0] + 2064), em = (n & 1) ? eb : ea;
      SCHED();
#pragma unroll
      for (int j = 0; j < 5; j++) ev[j] = e[2 * j] | e[2 * j + 1] << 16;
      ev[5] = e[10];
#pragma unroll
      for (int j = 0; j < 5; j++) od[j] = __builtin_amdgcn_alignbit(ev[j + 1], ev[j], 16);
      *(LDSP u32x4*)ob = (u32x4){ev[0], ev[1], ev[2], ev[3]};
      *(LDSP u32x4*)(ob + 4160) = (u32x4){od[0], od[1], od[2], od[3]};
      *(LDSP u32x4*)(ob + 8320) = (u32x4){ev[1], ev[2], ev[3], ev[4]};
      *(LDSP u32x4*)(ob + 12480) = (u32x4){od[1], od[2], od[3], od[4]};
      const u32 aa = ea | ea << 16, am = ea | em << 16, mb = em | eb << 16, bb = eb | eb << 16;
      *(LDSP u32x4*)(ob + 2048) = (u32x4){aa, aa, mb, bb};
      *(LDSP u32x4*)(ob + 2048 + 4160) = (u32x4){aa, am, bb, bb};
      *(LDSP u32x4*)(ob + 2048 + 8320) = (u32x4){aa, mb, bb, bb};
      *(LDSP u32x4*)(ob + 2048 + 12480) = (u32x4){am, bb, bb, bb};
      const u32 k01 = __builtin_amdgcn_readfirstlane(ev[0]), k12 = __builtin_amdgcn_readfirstlane(od[0]);
      if (n < 8)
        *(LDSP u32x4*)(ob - 32 + (n >> 1) * 4128) = (u32x4){0, 0, n == 7 ? k01 << 16 : 0u, n == 3 ? k01 << 16 : n == 5 ? k01 : n == 7 ? k12 : 0u};
    }
    STAMPI(6);
#pragma unroll 1
    for (int it = 0; it < SPIN_MAX; it++) {
      const bool b0 = ((gs[0][1] ^ tag) | (gs[0][3] ^ tag)) != 0, b1 = ((gs[1][1] ^ tag) | (gs[1][3] ^ tag)) != 0;
      const bool b2 = ((gmr[1] ^ tag) | (gmr[3] ^ tag)) != 0;
      if (!__builtin_amdgcn_ballot_w64(b0 | b1 | b2)) break;
      CFENCE();
      if (b0) gs[0] = ld16c(rw, tid * 16, WS_GS);
      if (b1) gs[1] = ld16c(rw, tid * 16, WS_GS + 8192);
      if (b2) gmr = ld16c(rw, gmo, WS_GM + cg * 32);
    }
    STAMPI(7);
    u32 gm[4][3];
    {
      u32 t = pk2(dadd(__uint_as_float(gmr[0]), 8), dadd(__uint_as_float(gmr[2]), 8));
      const float one = (lane & 2) ? 1.f : 0.f;
      t = pk2(lo2f(t) + one, hi2f(t) + one);
#pragma unroll
      for (int k = 0; k < 4; k++) gm[k][0] = __builtin_amdgcn_readlane(t, 2 * k), gm[k][2] = __builtin_amdgcn_readlane(t, 2 * k + 1);
    }
#define SEL(v, c) ((c) & 1 ? hi2f((v)[(c) & 2]) : lo2f((v)[(c) & 2]))
#define SELX(v, c) ((c) & 1 ? hi2f((v)[(c) >> 1]) : lo2f((v)[(c) >> 1]))
    u32 hp[2][4];
#pragma unroll
    for (int hf = 0; hf < 2; hf++) {
      const u32x4 sv = gs[hf];
      const f32x2 mm = {hi2f(sv[2]), hi2f(sv[0])}, rr = {__builtin_amdgcn_rcpf(lo2f(sv[2])), __builtin_amdgcn_rcpf(lo2f(sv[0]))};
#pragma unroll
      for (int c = 0; c < 4; c++) {
        const f32x2 h2 = (((f32x2){SELX(xr[2 * hf + 1], c), SELX(xr[2 * hf], c)} - mm) * rr) * SEL(gm[1], c) + SEL(gm[0], c);
        const u32 t = pk2(h2[0], h2[1]);
        hp[hf][c] = t;
        PIN(hp[hf][c]);
        *(LDSP u32*)(sm + C_HR + c * C_HP + (2046 - 1024 * hf - 2 * tid) * 2) = t;
      }
    }
    LDSP char* yr = sm + C_Y + ((tid * 8) ^ (tid & 48));
    PIN(yr);
    SCHED();
    if (WDP == 2 && w < 4) wd_load(wd, dso, dv);
    SCHED();
    STAMPI(8);
    LBAR();
    if (WDP == 3 && w < 4) wd_load(wd, dso, dv);
    SCHED();
    STAMPI(9);
    {
      asm volatile(
          "ds_read_b64 v[156:157], v90 offset:3584\n"
          "ds_read_b64 v[158:159], v90 offset:3592\n"
          "ds_read_b128 v[192:195], v91 offset:3584\n"
          "ds_read_b64 v[152:153], v90 offset:3072\n"
          "ds_read_b64 v[154:155], v90 offset:3080\n"
          "ds_read_b128 v[196:199], v91 offset:3072\n"
          "ds_read_b64 v[148:149], v90 offset:2560\n"
          "ds_read_b64 v[150:151], v90 offset:2568\n"
          "ds_read_b128 v[200:203], v91 offset:2560\n"
          "ds_read_b64 v[144:145], v90 offset:2048\n"
          "ds_read_b64 v[146:147], v90 offset:2056\n"
          "ds_read_b128 v[204:207], v91 offset:2048\n"
          "ds_read_b64 v[140:141], v90 offset:1536\n"
          "ds_read_b64 v[142:143], v90 offset:1544\n"
          "ds_read_b128 v[208:211], v91 offset:1536\n"
          "ds_read_b64 v[136:137], v90 offset:1024\n"
          "ds_read_b64 v[138:139], v90 offset:1032\n"
          "ds_read_b128 v[212:215], v91 offset:1024\n"
          "ds_read_b64 v[132:133], v90 offset:512\n"
          "ds_read_b64 v[134:135], v90 offset:520\n"
          "ds_read_b128 v[216:219], v91 offset:512\n"
          "ds_read_b64 v[128:129], v90 offset:0\n"
          "ds_read_b64 v[130:131], v90 offset:8\n"
          "ds_read_b128 v[220:223], v91 offset:0\n"
          "s_waitcnt lgkmcnt(15)\n"
          "v_mfma_f32_16x16x32_bf16 v[124:127], v[156:159], v[192:195], 0\n"
          "v_mfma_f32_16x16x32_bf16 v[120:123], v[152:155], v[192:195], 0\n"
          "v_mfma_f32_16x16x32_bf16 v[124:127], v[152:155], v[196:199], v[124:127]\n"
          "v_mfma_f32_16x16x32_bf16 v[116:119], v[148:151], v[192:195], 0\n"
          "v_mfma_f32_16x16x32_bf16 v[120:123], v[148:151], v[196:199], v[120:123]\n"
          "v_mfma_f32_16x16x32_bf16 v[124:127], v[148:151], v[200:203], v[124:127]\n"
          "s_waitcnt lgkmcnt(13)\n"
          "v_mfma_f32_16x16x32_bf16 v[112:115], v[144:147], v[192:195], 0\n"
          "v_mfma_f32_16x16x32_bf16 v[116:119], v[144:147], v[196:199], v[116:119]\n"
          "v_mfma_f32_16x16x32_bf16 v[120:123], v[144:147], v[200:203], v[120:123]\n"
          "s_waitcnt lgkmcnt(12)\n"
          "v_mfma_f32_16x16x32_bf16 v[124:127], v[144:147], v[204:207], v[124:127]\n"
          "s_waitcnt lgkmcnt(10)\n"
          "v_mfma_f32_16x16x32_bf16 v[108:111], v[140:143], v[192:195], 0\n"
          "ds_read_b64 v[188:189], v90 offset:3648\n"
          "ds_read_b64 v[190:191], v90 offset:3656\n"
          "v_mfma_f32_16x16x32_bf16 v[112:115], v[140:143], v[196:199], v[112:115]\n"
          "ds_read_b128 v[224:227], v91 offset:3648\n"
          "v_mfma_f32_16x16x32_bf16 v[116:119], v[140:143], v[200:203], v[116:119]\n"
          "ds_read_b64 v[184:185], v90 offset:3136\n"
          "ds_read_b64 v[186:187], v90 offset:3144\n"
          "v_mfma_f32_16x16x32_bf16 v[120:123], v[140:143], v[204:207], v[120:123]\n"
          "ds_read_b128 v[228:231], v91 offset:3136\n"
          "s_waitcnt lgkmcnt(15)\n"
          "v_mfma_f32_16x16x32_bf16 v[124:127], v[140:143], v[208:211], v[124:127]\n"
          "ds_read_b64 v[180:181], v90 offset:2624\n"
          "ds_read_b64 v[182:183], v90 offset:2632\n"
          "s_waitcnt lgkmcnt(15)\n"
          "v_mfma_f32_16x16x32_bf16 v[104:107], v[136:139], v[192:195], 0\n"
          "ds_read_b128 v[232:235], v91 offset:2624\n"
          "v_mfma_f32_16x16x32_bf16 v[108:111], v[136:139], v[196:199], v[108:111]\n"
          "ds_read_b64 v[176:177], v90 offset:2112\n"
          "ds_read_b64 v[178:179], v90 offset:2120\n"
          "v_mfma_f32_16x16x32_bf16 v[112:115], v[136:139], v[200:203], v[112:115]\n"
          "ds_read_b128 v[236:239], v91 offset:2112\n"
          "v_mfma_f32_16x16x32_bf16 v[116:119], v[136:139], v[204:207], v[116:119]\n"
          "ds_read_b64 v[172:173], v90 offset:1600\n"
          "ds_read_b64 v[174:175], v90 offset:1608\n"
          "v_mfma_f32_16x16x32_bf16 v[120:123], v[136:139], v[208:211], v[120:123]\n"
          "ds_read_b128 v[240:243], v91 offset:1600\n"
          "s_waitcnt lgkmcnt(15)\n"
          "v_mfma_f32_16x16x32_bf16 v[124:127], v[136:139], v[212:215], v[124:127]\n"
          "ds_read_b64 v[168:169], v90 offset:1088\n"
          "ds_read_b64 v[170:171], v90 offset:1096\n"
          "v_mfma_f32_16x16x32_bf16 v[100:103], v[132:135], v[192:195], 0\n"
          "ds_read_b128 v[244:247], v91 offset:1088\n"
          "v_mfma_f32_16x16x32_bf16 v[104:107], v[132:135], v[196:199], v[104:107]\n"
          "ds_read_b64 v[164:165], v90 offset:576\n"
          "ds_read_b64 v[166:167], v90 offset:584\n"
          "v_mfma_f32_16x16x32_bf16 v[108:111], v[132:135], v[200:203], v[108:111]\n"
          "ds_read_b128 v[248:251], v91 offset:576\n"
          "v_mfma_f32_16x16x32_bf16 v[112:115], v[132:135], v[204:207], v[112:115]\n"
          "ds_read_b64 v[160:161], v90 offset:64\n"
          "ds_read_b64 v[162:163], v90 offset:72\n"
          "v_mfma_f32_16x16x32_bf16 v[116:119], v[132:135], v[208:211], v[116:119]\n"
          "ds_read_b128 v[252:255], v91 offset:64\n"
          "v_mfma_f32_16x16x32_bf16 v[120:123], v[132:135], v[212:215], v[120:123]\n"
          "v_mfma_f32_16x16x32_bf16 v[124:127], v[132:135], v[216:219], v[124:127]\n"
          "v_mfma_f32_16x16x32_bf16 v[96:99], v[128:131], v[192:195], 0\n"
          "v_mfma_f32_16x16x32_bf16 v[100:103], v[128:131], v[196:199], v[100:103]\n"
          "v_mfma_f32_16x16x32_bf16 v[104:107], v[128:131], v[200:203], v[104:107]\n"
          "v_mfma_f32_16x16x32_bf16 v[108:111], v[128:131], v[204:207], v[108:111]\n"
          "v_mfma_f32_16x16x32_bf16 v[112:115], v[128:131], v[208:211], v[112:115]\n"
          "v_mfma_f32_16x16x32_bf16 v[116:119], v[128:131], v[212:215], v[116:119]\n"
          "v_mfma_f32_16x16x32_bf16 v[120:123], v[128:131], v[216:219], v[120:123]\n"
          "v_mfma_f32_16x16x32_bf16 v[124:127], v[128:131], v[220:223], v[124:127]\n"
          "s_waitcnt lgkmcnt(15)\n"
          "v_mfma_f32_16x16x32_bf16 v[124:127], v[188:191], v[224:227], v[124:127]\n"
          "v_mfma_f32_16x16x32_bf16 v[120:123], v[184:187], v[224:227], v[120:123]\n"
          "v_mfma_f32_16x16x32_bf16 v[124:127], v[184:187], v[228:231], v[124:127]\n"
          "v_mfma_f32_16x16x32_bf16 v[116:119], v[180:183], v[224:227], v[116:119]\n"
          "v_mfma_f32_16x16x32_bf16 v[120:123], v[180:183], v[228:231], v[120:123]\n"
          "ds_read_b64 v[156:157], v90 offset:3712\n"
          "ds_read_b64 v[158:159], v90 offset:3720\n"
          "v_mfma_f32_16x16x32_bf16 v[124:127], v[180:183], v[232:235], v[124:127]\n"
          "s_waitcnt lgkmcnt(15)\n"
          "v_mfma_f32_16x16x32_bf16 v[112:115], v[176:179], v[224:227], v[112:115]\n"
          "ds_read_b128 v[192:195], v91 offset:3712\n"
          "v_mfma_f32_16x16x32_bf16 v[116:119], v[176:179], v[228:231], v[116:119]\n"
          "v_mfma_f32_16x16x32_bf16 v[120:123], v[176:179], v[232:235], v[120:123]\n"
          "ds_read_b64 v[152:153], v90 offset:3200\n"
          "ds_read_b64 v[154:155], v90 offset:3208\n"
          "s_waitcnt lgkmcnt(15)\n"
          "v_mfma_f32_16x16x32_bf16 v[124:127], v[176:179], v[236:239], v[124:127]\n"
          "v_mfma_f32_16x16x32_bf16 v[108:111], v[172:175], v[224:227], v[108:111]\n"
          "ds_read_b128 v[196:199], v91 offset:3200\n"
          "v_mfma_f32_16x16x32_bf16 v[112:115], v[172:175], v[228:231], v[112:115]\n"
          "v_mfma_f32_16x16x32_bf16 v[116:119], v[172:175], v[232:235], v[116:119]\n"
          "ds_read_b64 v[148:149], v90 offset:2688\n"
          "ds_read_b64 v[150:151], v90 offset:2696\n"
          "v_mfma_f32_16x16x32_bf16 v[120:123], v[172:175], v[236:239], v[120:123]\n"
          "s_waitcnt lgkmcnt(15)\n"
          "v_mfma_f32_16x16x32_bf16 v[124:127], v[172:175], v[240:243], v[124:127]\n"
          "ds_read_b128 v[200:203], v91 offset:2688\n"
          "v_mfma_f32_16x16x32_bf16 v[104:107], v[168:171], v[224:227], v[104:107]\n"
          "v_mfma_f32_16x16x32_bf16 v[108:111], v[168:171], v[228:231], v[108:111]\n"
          "ds_read_b64 v[144:145], v90 offset:2176\n"
          "ds_read_b64 v[146:147], v90 offset:2184\n"
          "v_mfma_f32_16x16x32_bf16 v[112:115], v[168:171], v[232:235], v[112:115]\n"
          "v_mfma_f32_16x16x32_bf16 v[116:119], v[168:171], v[236:239], v[116:119]\n"
          "ds_read_b128 v[204:207], v91 offset:2176\n"
          "v_mfma_f32_16x16x32_bf16 v[120:123], v[168:171], v[240:243], v[120:123]\n"
          "s_waitcnt lgkmcnt(15)\n"
          "v_mfma_f32_16x16x32_bf16 v[124:127], v[168:171], v[244:247], v[124:127]\n"
          "ds_read_b64 v[140:141], v90 offset:1664\n"
          "ds_read_b64 v[142:143], v90 offset:1672\n"
          "v_mfma_f32_16x16x32_bf16 v[100:103], v[164:167], v[224:227], v[100:103]\n"
          "v_mfma_f32_16x16x32_bf16 v[104:107], v[164:167], v[228:231], v[104:107]\n"
          "ds_read_b128 v[208:211], v91 offset:1664\n"
          "v_mfma_f32_16x16x32_bf16 v[108:111], v[164:167], v[232:235], v[108:111]\n"
          "v_mfma_f32_16x16x32_bf16 v[112:115], v[164:167], v[236:239], v[112:115]\n"
          "ds_read_b64 v[136:137], v90 offset:1152\n"
          "ds_read_b64 v[138:139], v90 offset:1160\n"
          "v_mfma_f32_16x16x32_bf16 v[116:119], v[164:167], v[240:243], v[116:119]\n"
          "v_mfma_f32_16x16x32_bf16 v[120:123], v[164:167], v[244:247], v[120:123]\n"
          "ds_read_b128 v[212:215], v91 offset:1152\n"
          "v_mfma_f32_16x16x32_bf16 v[124:127], v[164:167], v[248:251], v[124:127]\n"
          "s_waitcnt lgkmcnt(15)\n"
          "v_mfma_f32_16x16x32_bf16 v[96:99], v[160:163], v[224:227], v[96:99]\n"
          "ds_read_b64 v[132:133], v90 offset:640\n"
          "ds_read_b64 v[134:135], v90 offset:648\n"
          "v_mfma_f32_16x16x32_bf16 v[100:103], v[160:163], v[228:231], v[100:103]\n"
          "v_mfma_f32_16x16x32_bf16 v[104:107], v[160:163], v[232:235], v[104:107]\n"
          "ds_read_b128 v[216:219], v91 offset:640\n"
          "v_mfma_f32_16x16x32_bf16 v[108:111], v[160:163], v[236:239], v[108:111]\n"
          "ds_read_b64 v[128:129], v90 offset:128\n"
          "ds_read_b64 v[130:131], v90 offset:136\n"
          "v_mfma_f32_16x16x32_bf16 v[112:115], v[160:163], v[240:243], v[112:115]\n"
          "ds_read_b128 v[220:223], v91 offset:128\n"
          "v_mfma_f32_16x16x32_bf16 v[116:119], v[160:163], v[244:247], v[116:119]\n"
          "v_mfma_f32_16x16x32_bf16 v[120:123], v[160:163], v[248:251], v[120:123]\n"
          "v_mfma_f32_16x16x32_bf16 v[124:127], v[160:163], v[252:255], v[124:127]\n"
          "v_mfma_f32_16x16x32_bf16 v[124:127], v[156:159], v[192:195], v[124:127]\n"
          "s_waitcnt lgkmcnt(15)\n"
          "v_mfma_f32_16x16x32_bf16 v[120:123], v[152:155], v[192:195], v[120:123]\n"
          "v_mfma_f32_16x16x32_bf16 v[124:127], v[152:155], v[196:199], v[124:127]\n"
          "v_mfma_f32_16x16x32_bf16 v[116:119], v[148:151], v[192:195], v[116:119]\n"
          "v_mfma_f32_16x16x32_bf16 v[120:123], v[148:151], v[196:199], v[120:123]\n"
          "ds_read_b64 v[160:161], v90 offset:192\n"
          "ds_read_b64 v[162:163], v90 offset:200\n"
          "v_mfma_f32_16x16x32_bf16 v[124:127], v[148:151], v[200:203], v[124:127]\n"
          "s_waitcnt lgkmcnt(15)\n"
          "v_mfma_f32_16x16x32_bf16 v[112:115], v[144:147], v[192:195], v[112:115]\n"
          "ds_read_b128 v[252:255], v91 offset:192\n"
          "v_mfma_f32_16x16x32_bf16 v[116:119], v[144:147], v[196:199], v[116:119]\n"
          "v_mfma_f32_16x16x32_bf16 v[120:123], v[144:147], v[200:203], v[120:123]\n"
          "ds_read_b128 v[248:251], v91 offset:704\n"
          "s_waitcnt lgkmcnt(15)\n"
          "v_mfma_f32_16x16x32_bf16 v[124:127], v[144:147], v[204:207], v[124:127]\n"
          "s_waitcnt lgkmcnt(14)\n"
          "v_mfma_f32_16x16x32_bf16 v[108:111], v[140:143], v[192:195], v[108:111]\n"
          "ds_read_b64 v[164:165], v90 offset:704\n"
          "ds_read_b64 v[166:167], v90 offset:712\n"
          "v_mfma_f32_16x16x32_bf16 v[112:115], v[140:143], v[196:199], v[112:115]\n"
          "v_mfma_f32_16x16x32_bf16 v[116:119], v[140:143], v[200:203], v[116:119]\n"
          "ds_read_b128 v[244:247], v91 offset:1216\n"
          "v_mfma_f32_16x16x32_bf16 v[120:123], v[140:143], v[204:207], v[120:123]\n"
          "s_waitcnt lgkmcnt(15)\n"
          "v_mfma_f32_16x16x32_bf16 v[124:127], v[140:143], v[208:211], v[124:127]\n"
          "ds_read_b64 v[168:169], v90 offset:1216\n"
          "ds_read_b64 v[170:171], v90 offset:1224\n"
          "s_waitcnt lgkmcnt(15)\n"
          "v_mfma_f32_16x16x32_bf16 v[104:107], v[136:139], v[192:195], v[104:107]\n"
          "v_mfma_f32_16x16x32_bf16 v[108:111], v[136:139], v[196:199], v[108:111]\n"
          "ds_read_b128 v[240:243], v91 offset:1728\n"
          "v_mfma_f32_16x16x32_bf16 v[112:115], v[136:139], v[200:203], v[112:115]\n"
          "v_mfma_f32_16x16x32_bf16 v[116:119], v[136:139], v[204:207], v[116:119]\n"
          "ds_read_b64 v[172:173], v90 offset:1728\n"
          "ds_read_b64 v[174:175], v90 offset:1736\n"
          "v_mfma_f32_16x16x32_bf16 v[120:123], v[136:139], v[208:211], v[120:123]\n"
          "v_mfma_f32_16x16x32_bf16 v[124:127], v[136:139], v[212:215], v[124:127]\n"
          "ds_read_b128 v[236:239], v91 offset:2240\n"
          "s_waitcnt lgkmcnt(15)\n"
          "v_mfma_f32_16x16x32_bf16 v[100:103], v[132:135], v[192:195], v[100:103]\n"
          "v_mfma_f32_16x16x32_bf16 v[104:107], v[132:135], v[196:199], v[104:107]\n"
          "ds_read_b64 v[176:177], v90 offset:2240\n"
          "ds_read_b64 v[178:179], v90 offset:2248\n"
          "v_mfma_f32_16x16x32_bf16 v[108:111], v[132:135], v[200:203], v[108:111]\n"
          "v_mfma_f32_16x16x32_bf16 v[112:115], v[132:135], v[204:207], v[112:115]\n"
          "ds_read_b128 v[232:235], v91 offset:2752\n"
          "v_mfma_f32_16x16x32_bf16 v[116:119], v[132:135], v[208:211], v[116:119]\n"
          "v_mfma_f32_16x16x32_bf16 v[120:123], v[132:135], v[212:215], v[120:123]\n"
          "ds_read_b64 v[180:181], v90 offset:2752\n"
          "ds_read_b64 v[182:183], v90 offset:2760\n"
          "v_mfma_f32_16x16x32_bf16 v[124:127], v[132:135], v[216:219], v[124:127]\n"
          "s_waitcnt lgkmcnt(15)\n"
          "v_mfma_f32_16x16x32_bf16 v[96:99], v[128:131], v[192:195], v[96:99]\n"
          "ds_read_b128 v[228:231], v91 offset:3264\n"
          "v_mfma_f32_16x16x32_bf16 v[100:103], v[128:131], v[196:199], v[100:103]\n"
          "v_mfma_f32_16x16x32_bf16 v[104:107], v[128:131], v[200:203], v[104:107]\n"
          "ds_read_b64 v[184:185], v90 offset:3264\n"
          "ds_read_b64 v[186:187], v90 offset:3272\n"
          "v_mfma_f32_16x16x32_bf16 v[108:111], v[128:131], v[204:207], v[108:111]\n"
          "ds_read_b128 v[224:227], v91 offset:3776\n"
          "v_mfma_f32_16x16x32_bf16 v[112:115], v[128:131], v[208:211], v[112:115]\n"
          "ds_read_b64 v[188:189], v90 offset:3776\n"
          "ds_read_b64 v[190:191], v90 offset:3784\n"
          "v_mfma_f32_16x16x32_bf16 v[116:119], v[128:131], v[212:215], v[116:119]\n"
          "v_mfma_f32_16x16x32_bf16 v[120:123], v[128:131], v[216:219], v[120:123]\n"
          "v_mfma_f32_16x16x32_bf16 v[124:127], v[128:131], v[220:223], v[124:127]\n"
          "v_mfma_f32_16x16x32_bf16 v[124:127], v[160:163], v[252:255], v[124:127]\n"
          "s_waitcnt lgkmcnt(15)\n"
          "v_mfma_f32_16x16x32_bf16 v[120:123], v[160:163], v[248:251], v[120:123]\n"
          "v_mfma_f32_16x16x32_bf16 v[124:127], v[164:167], v[248:251], v[124:127]\n"
          "v_mfma_f32_16x16x32_bf16 v[120:123], v[164:167], v[244:247], v[120:123]\n"
          "v_mfma_f32_16x16x32_bf16 v[124:127], v[168:171], v[244:247], v[124:127]\n"
          "s_waitcnt lgkmcnt(14)\n"
          "v_mfma_f32_16x16x32_bf16 v[120:123], v[168:171], v[240:243], v[120:123]\n"
          "s_waitcnt lgkmcnt(12)\n"
          "v_mfma_f32_16x16x32_bf16 v[124:127], v[172:175], v[240:243], v[124:127]\n"
          "s_waitcnt lgkmcnt(11)\n"
          "v_mfma_f32_16x16x32_bf16 v[120:123], v[172:175], v[236:239], v[120:123]\n"
          "s_waitcnt lgkmcnt(9)\n"
          "v_mfma_f32_16x16x32_bf16 v[124:127], v[176:179], v[236:239], v[124:127]\n"
          "s_waitcnt lgkmcnt(8)\n"
          "v_mfma_f32_16x16x32_bf16 v[120:123], v[176:179], v[232:235], v[120:123]\n"
          "s_waitcnt lgkmcnt(6)\n"
          "v_mfma_f32_16x16x32_bf16 v[124:127], v[180:183], v[232:235], v[124:127]\n"
          "s_waitcnt lgkmcnt(5)\n"
          "v_mfma_f32_16x16x32_bf16 v[120:123], v[180:183], v[228:231], v[120:123]\n"
          "s_waitcnt lgkmcnt(3)\n"
          "v_mfma_f32_16x16x32_bf16 v[124:127], v[184:187], v[228:231], v[124:127]\n"
          "s_waitcnt lgkmcnt(2)\n"
          "v_mfma_f32_16x16x32_bf16 v[120:123], v[184:187], v[224:227], v[120:123]\n"
          "s_waitcnt lgkmcnt(0)\n"
          "v_mfma_f32_16x16x32_bf16 v[124:127], v[188:191], v[224:227], v[124:127]\n"
          "v_mfma_f32_16x16x32_bf16 v[116:119], v[160:163], v[244:247], v[116:119]\n"
          "v_mfma_f32_16x16x32_bf16 v[112:115], v[160:163], v[240:243], v[112:115]\n"
          "v_mfma_f32_16x16x32_bf16 v[108:111], v[160:163], v[236:239], v[108:111]\n"
          "v_mfma_f32_16x16x32_bf16 v[116:119], v[164:167], v[240:243], v[116:119]\n"
          "v_mfma_f32_16x16x32_bf16 v[112:115], v[164:167], v[236:239], v[112:115]\n"
          "s_nop 0\n"
          "ds_write_b128 v92, v[120:123] offset:6144\n"
          "v_mfma_f32_16x16x32_bf16 v[108:111], v[164:167], v[232:235], v[108:111]\n"
          "v_mfma_f32_16x16x32_bf16 v[116:119], v[168:171], v[236:239], v[116:119]\n"
          "ds_write_b128 v92, v[124:127] offset:7168\n"
          "v_mfma_f32_16x16x32_bf16 v[112:115], v[168:171], v[232:235], v[112:115]\n"
          "v_mfma_f32_16x16x32_bf16 v[108:111], v[168:171], v[228:231], v[108:111]\n"
          "v_mfma_f32_16x16x32_bf16 v[116:119], v[172:175], v[232:235], v[116:119]\n"
          "v_mfma_f32_16x16x32_bf16 v[112:115], v[172:175], v[228:231], v[112:115]\n"
          "v_mfma_f32_16x16x32_bf16 v[108:111], v[172:175], v[224:227], v[108:111]\n"
          "v_mfma_f32_16x16x32_bf16 v[116:119], v[176:179], v[228:231], v[116:119]\n"
          "v_mfma_f32_16x16x32_bf16 v[112:115], v[176:179], v[224:227], v[112:115]\n"
          "v_mfma_f32_16x16x32_bf16 v[116:119], v[180:183], v[224:227], v[116:119]\n"
          "v_mfma_f32_16x16x32_bf16 v[104:107], v[160:163], v[232:235], v[104:107]\n"
          "v_mfma_f32_16x16x32_bf16 v[100:103], v[160:163], v[228:231], v[100:103]\n"
          "v_mfma_f32_16x16x32_bf16 v[96:99], v[160:163], v[224:227], v[96:99]\n"
          "s_nop 1\n"
          "ds_write_b128 v92, v[108:111] offset:3072\n"
          "v_mfma_f32_16x16x32_bf16 v[104:107], v[164:167], v[228:231], v[104:107]\n"
          "v_mfma_f32_16x16x32_bf16 v[100:103], v[164:167], v[224:227], v[100:103]\n"
          "ds_write_b128 v92, v[112:115] offset:4096\n"
          "v_mfma_f32_16x16x32_bf16 v[104:107], v[168:171], v[224:227], v[104:107]\n"
          "ds_write_b128 v92, v[116:119] offset:5120\n"
          "s_nop 6\n"
          "ds_write_b128 v92, v[104:107] offset:2048\n"
          "ds_write_b128 v92, v[100:103] offset:1024\n"
          "ds_write_b128 v92, v[96:99] offset:0\n"
          "s_waitcnt lgkmcnt(0)\n"
          :: "{v90}"(pu), "{v91}"(pv), "{v92}"(py)
          : "v96", "v97", "v98", "v99", "v100", "v101", "v102", "v103", "v104", "v105", "v106", "v107", "v108", "v109", "v110", "v111", "v112", "v113", "v114", "v115", "v116", "v117", "v118", "v119", "v120", "v121", "v122", "v123", "v124", "v125", "v126", "v127", "v128", "v129", "v130", "v131", "v132", "v133", "v134", "v135", "v136", "v137", "v138", "v139", "v140", "v141", "v142", "v143", "v144", "v145", "v146", "v147", "v148", "v149", "v150", "v151", "v152", "v153", "v154", "v155", "v156", "v157", "v158", "v159", "v160", "v161", "v162", "v163", "v164", "v165", "v166", "v167", "v168", "v169", "v170", "v171", "v172", "v173", "v174", "v175", "v176", "v177", "v178", "v179", "v180", "v181", "v182", "v183", "v184", "v185", "v186", "v187", "v188", "v189", "v190", "v191", "v192", "v193", "v194", "v195", "v196", "v197", "v198", "v199", "v200", "v201", "v202", "v203", "v204", "v205", "v206", "v207", "v208", "v209", "v210", "v211", "v212", "v213", "v214", "v215", "v216", "v217", "v218", "v219", "v220", "v221", "v222", "v223", "v224", "v225", "v226", "v227", "v228", "v229", "v230", "v231", "v232", "v233", "v234", "v235", "v236", "v237", "v238", "v239", "v240", "v241", "v242", "v243", "v244", "v245", "v246", "v247", "v248", "v249", "v250", "v251", "v252", "v253", "v254", "v255", "memory");
    }
    STAMPI(10);
    if (WDP == 4 && w < 4) wd_load(wd, dso, dv);
    if (WDP != 4 && w < 4) wd_store(rw, dst, dv, lane);
    STAMPI(12);
    BAR();
    STAMPI(11);
#pragma unroll
    for (int hf = 0; hf < 2; hf++) {
      float o0[4], o1[4];
#pragma unroll
      for (int c = 0; c < 4; c++) {
        const LDSP char* pr = yr + c * 8192 + hf * 4096;
        const f32x2 y = *(const LDSP f32x2*)pr + *(const LDSP f32x2*)(pr + 32768);
        const f32x2 t = (f32x2){hi2f(hp[hf][c]), lo2f(hp[hf][c])} * SELX(dkp, c) + y;
        const f32x2 o = t * SEL(gm[2], c) + (f32x2){SELX(xr[2 * hf], c), SELX(xr[2 * hf + 1], c)};
        o0[c] = o[0];
        o1[c] = o[1];
      }
      const u32x4 xo = {pk2(o0[0], o0[1]), pk2(o0[2], o0[3]), pk2(o1[0], o1[1]), pk2(o1[2], o1[3])};
      u32x4 xs;
#pragma unroll
      for (int i = 0; i < 4; i++) xs[i] = pk2(lo2f(xo[i]) * SEL(gm[3], (2 * i) & 3), hi2f(xo[i]) * SEL(gm[3], (2 * i + 1) & 3));
      st16(rw, WS_X1S + cg * 16384 + hf * 8192 + tid * 16, xs);
      st16(rw, WS_X1T + cg * 16384 + hf * 8192 + tid * 16, xo);
    }
    SCHED();
    if (WDP == 4 && w < 4) wd_store(rw, dst, dv, lane);
  } else {
    const int p = b - 144, u = p;
    const bool act = u < 108;
    CFENCE();
    u32 tag = __builtin_amdgcn_raw_buffer_load_b32(rw, 0, WS_EP, 16);
    const int tb = act ? 896 + u : 992 + (u - 108) * 8 + w;
    const bool hasb = act ? u < 96 && w == 0 : true;
    const int S = u / 3, p3 = u - 3 * S, jj = S / 9, tin = S - 9 * jj, m = w & 1;
    auto getmod = [&](u32 off) {
      u32x4 g0, g1;
#pragma unroll 1
      for (int it = 0; it < SPIN_MAX; it++) {
        g0 = ld16c(rw, off, WS_GM);
        g1 = ld16c(rw, off, WS_GM + 27648);
        if (!__builtin_amdgcn_ballot_w64(((g0[1] ^ tag) | (g0[3] ^ tag) | (g1[1] ^ tag) | (g1[3] ^ tag)) != 0)) break;
        __builtin_amdgcn_s_sleep(1);
        CFENCE();
      }
      return pk2(__uint_as_float(g0[0]) + __uint_as_float(g1[0]), __uint_as_float(g0[2]) + __uint_as_float(g1[2]));
    };
    u32x4 xv[3], xw[3], mv[5], wt[2][8];
    u32 dso, dst;
    {
      ln_load(rx, u * 8 + w, lane, xv);
      if (hasb) ln_load(rx, tb, lane, xw);
      SCHED();
      const rsrc_t rm = mkrs(wmod + (u & 1) * 288 * 3456, act ? 288 * 3456 * 2 : 0), rc = mkrs(cvec + (u & 1) * 288, 576);
      const u32 r0 = 36 * w + 4 * (lane >> 3), r4 = lane < 32 ? 36 * w + 32 + (lane >> 3) : 288;
      const u32x2 c03 = ld8(rc, r0 * 2, 0);
      const u16 c4 = __builtin_amdgcn_raw_buffer_load_b16(rc, r4 * 2, 0, 0);
#pragma unroll
      for (int i = 0; i < 5; i++) mv[i] = ld16n(rm, (i < 4 ? r0 : r4) * 6912 + (lane & 7) * 16, (u >> 1) * 128 + (i & 3) * 6912);
      SCHED();
      auto wload = [&](int tk, int j0, int j1) {
        const rsrc_t rg = mkrs(m ? wu : wg, D_ * F_ * 2);
        const int q = 6 * p3 + (w >> 1) + 4 * tk;
#pragma unroll
        for (int j = j0; j < j1; j++) wt[tk][j] = ld16n(rg, 16 * (lane & 15) + 16 * (lane >> 4) * F_, 256 * S + (32 * q + j) * F_ * 2);
      };
      u32 tl = 0;
      if (PF && act && lane < 64 / PF)
        tl = __builtin_amdgcn_raw_buffer_load_b32(mkrs(m ? wu : wg, D_ * F_ * 2), (((lane * PF) & 31) + 128 * ((lane * PF) >> 5)) * F_ * 2, 256 * S + 32 * (6 * p3 + (w >> 1)) * F_ * 2, 0);
      SCHED();
      if (EBP == 0) { BAR(); if (act) wload(0, 0, EB); SCHED(); }
      STAMPI(1);
      MKTAG();
      ln_stat(rw, u * 8 + w, lane, xv, tag);
      if (hasb) ln_stat(rw, tb, lane, xw, tag);
      if (EBP == 1) { SCHED(); if (act) wload(0, 0, EB); SCHED(); }
      STAMPI(2);
      f32x2 acc[4] = {{0, 0}, {0, 0}, {0, 0}, {0, 0}};
#pragma unroll
      for (int i = 0; i < 5; i++) {
        const float c = i == 4 ? b2f(c4) : (i & 1) ? hi2f(c03[i >> 1]) : lo2f(c03[i >> 1]), s = rb(c * __builtin_amdgcn_rcpf(1.f + __expf(-c)));
#pragma unroll
        for (int j = 0; j < 4; j++) acc[j] += s * (f32x2){lo2f(mv[i][j]), hi2f(mv[i][j])};
      }
      f32x2 r2;
#pragma unroll
      for (int k = 0; k < 2; k++) r2[k] = dadd(sw16(sw32(acc[0][k], acc[2][k]), sw32(acc[1][k], acc[3][k])), 8);
      if ((lane & 8) == 0) *(LDSP f32x2*)(sm + w * 256 + (lane & 7) * 32 + (lane >> 4) * 8) = r2;
      LBAR();
      if (tid == 0) st4(rw, WS_ARR + b * 4, tag);
      if (w == 7 && act) {
        float s = 0.f;
#pragma unroll
        for (int i = 0; i < 8; i++) s += *(LDSP float*)(sm + i * 256 + lane * 4);
        st8(rw, WS_GM + (u & 1) * 27648 + (u >> 1) * 512 + lane * 8, (u32x2){__float_as_uint(s), tag});
      }
      PIN(tl);
      STAMPI(3);
      SCHED();
      if (act && !GATE) {
        if (DLY) __builtin_amdgcn_s_sleep(DLY);
        wload(0, EB, 8);
        if (w < 4) wload(1, 0, 8);
        else if (w == 4 && u < 72) { wd_addr(576 + u, lane, dso, dst); wd_load(wd, dso, wt[1]); }
      }
      if (act) {
      if (w > 4) {
        const int i = tid - 320, vec = i >= 96;
        u32 t = getmod((4 - vec) * 4608 + p3 * 1536 + (i - 96 * vec) * 16);
        if (!vec) t = pk2(lo2f(t) + 1.f, hi2f(t) + 1.f);
        *(LDSP u32*)(sm + 12288 + i * 4) = t;
      } else if (w == 4) {
        *(LDSP u32x2*)(sm + 12288 + 768 + lane * 8) = (u32x2){0, 0};
      }
      LBAR();
        if (GATE) wload(0, EB, 8);
        if (GATE && w < 4) wload(1, 0, 8);
        else if (GATE && w == 4 && u < 72) { wd_addr(576 + u, lane, dso, dst); wd_load(wd, dso, wt[1]); }
      }
      SCHED();
      STAMPI(7);
    }
    if (act) {
      u32x4 fr[2][8];
#pragma unroll
      for (int tk = 0; tk < 2; tk++)
        if (tk == 0 || w < 4) {
          const int q = 6 * p3 + (w >> 1) + 4 * tk;
          const int f1 = tin == 0 ? 90 + 2 * q : 34 + 6 * q + 2 * tin, f2 = q < 15 ? 8 * q + 2 * tin : f1, fidx = m + (tin >= 4 ? 36 * tin + 2 * q : f2);
#pragma unroll
          for (int i = 0; i < 8; i++) {
            fr[tk][i] = tcol(wt[tk], i);
            st16b(rw, WS_WP + ((8 * jj + i) * 324 + fidx) * 1024 + lane * 16, fr[tk][i]);
          }
        }
      STAMPI(4);
      if (w == 4 && u < 72) wd_store(rw, dst, wt[1], lane);
      f32x4 cs[8];
#pragma unroll
      for (int tk = 0; tk < 2; tk++)
        if (tk == 0 || w < 4) {
          const bf16x8 bv = *(const LDSP bf16x8*)(sm + 12288 + ((lane & 15) < 2 ? lane & 15 : 2) * 384 + ((w >> 1) + 4 * tk) * 64 + (lane >> 4) * 16);
#pragma unroll
          for (int i = 0; i < 8; i++) cs[i] = MFMA16(asb(fr[tk][i]), bv, tk ? cs[i] : ((f32x4){0, 0, 0, 0}));
        }
      if ((lane & 15) < 2) {
#pragma unroll
        for (int i = 0; i < 8; i++) *(LDSP f32x4*)(sm + 4096 + w * 1024 + i * 128 + (lane & 15) * 64 + (lane >> 4) * 16) = cs[i];
      }
      STAMPI(5);
      STAMPI(6);
      LBAR();
      {
        const LDSP float* pc = (const LDSP float*)(sm + 4096 + ((lane >> 1) & 1) * 1024 + w * 128 + (lane & 1) * 64 + (lane >> 2) * 4);
        st4(rw, WS_CSB + ((8 * jj + w) * 3 + p3) * 2304 + tin * 256 + lane * 4, __float_as_uint((pc[0] + pc[512]) + (pc[1024] + pc[1536])));
      }
    } else {
      if (u < 110) st16(rw, WS_ST2 + (u - 108) * 8192 + tid * 16, (u32x4){~0u, ~0u, ~0u, ~0u});
      if (u == 110 && tid < 64) st16(rw, WS_FLG + tid * 16, (u32x4){0, 0, 0, 0});
      if (u == 110 && w < 5) {
        const u32 i = tid < 288 ? tid : 0, t = getmod(5 * 4608 + i * 16);
        st8(rw, WS_MODF + 5 * 2304 + i * 8, (u32x2){t << 16, t & 0xffff0000u});
      }
      if (u == 111 && w == 0) {
        bool ok = false;
#pragma unroll 1
        for (int it = 0; it < SPIN_MAX && !ok; it++) {
          const u32x4 a = ld16c(rw, lane * 16, WS_ARR);
          ok = !__builtin_amdgcn_ballot_w64(((a[0] ^ tag) | (a[1] ^ tag) | (a[2] ^ tag) | (a[3] ^ tag)) != 0);
          __builtin_amdgcn_s_sleep(2);
          CFENCE();
        }
        if (lane == 0) st4(rw, WS_EP, tag);
      }
    }
  }
  STAMP_END(0);
}

__global__ __launch_bounds__(512, 2) void k3(char* ws, u16* out) {
  __shared__ __attribute__((aligned(16))) char smem[160000];
  STAMP_BEGIN();
  const u32 b = blockIdx.x, w = __builtin_amdgcn_readfirstlane(threadIdx.x >> 6);
  const u32 xcd = b & 7, idx = b >> 3;
  const u32 tt = (xcd & 1) * 4 + (idx & 3), fb = (xcd >> 1) * 8 + (idx >> 2);
  const u32 j = xcd >> 1, tb = (xcd & 1) * 8 + (idx & 7), db = idx >> 3, tg = w & 3, kh = w >> 2, T = 2 * j + kh;
  const u32 peer = (idx << 3) | (xcd & 1);
  u32x16 A, B;
  A[0] = (u32)(uintptr_t)ws; A[1] = (u32)((uintptr_t)ws >> 32) & 0xffff; A[2] = WS_END; A[3] = 0x20000;
  A[4] = (u32)(uintptr_t)out; A[5] = (u32)((uintptr_t)out >> 32) & 0xffff; A[6] = L_ * D_ * 2; A[7] = 0x20000;
  A[8] = WS_WP + (fb * 324 + w) * 1024;
  A[9] = w * 1024 + (u32)(uintptr_t)(LDSP char*)smem;
  A[10] = WS_X1S + (tt * 256 + w * 32) * 8;
  A[11] = WS_X1T + w * 18 * 16384 + (tt * 256 + fb * 8) * 8;
  A[12] = WS_HT + ((tt * 8 + w) * 288 + 72 * (fb >> 3) + (fb & 7)) * 1024;
  A[13] = WS_ST2 + (tt * 256 + w * 32) * 8;
  A[14] = WS_ST2 + (tt * 256 + fb * 8) * 8;
  A[15] = WS_CSB + fb * 6912 + w * 1024;
  B[0] = WS_WD + ((j * 4 + db) * 324 + w) * 1024 - 324 * 1024;
  B[1] = (w < 4 ? A[8] : B[0]) + 40 * 8192;
  B[2] = WS_FLG + (tt * 32 + fb) * 4;
  B[3] = WS_FLG + ((tb >> 1) * 32 + 8 * j + 4 * kh) * 4;
  B[4] = WS_HT + ((4 * tb + tg) * 288 + 72 * j + 4 * kh) * 1024;
#pragma unroll
  for (u32 a = 0; a < 3; a++) B[5 + a] = WS_XCH + (peer + 2 * ((j + 1 + a) & 3)) * 32768 + a * 8192 + w * 1024;
  B[8] = WS_XCH + (peer + 2 * tg) * 32768 + 24576 + ((tg - j - 1) & 3) * 1024;
  B[9] = WS_XCH + b * 32768 + w * 1024;
  B[10] = kh ? 0 : tg == j ? 2 : 1;
  B[11] = WS_X1T + (36 * db + 4 * T) * 16384 + (tb * 128 + tg * 32) * 8;
  B[12] = WS_MODF + (5 * 576 + 144 * db + 16 * T) * 4;
  B[13] = ((tb * 128 + tg * 32) * 576 + 144 * db + 16 * T) * 2;
  B[14] = WS_XCH + b * 32768 + 24576;
  B[15] = 8 - T;
  u32 vl = (threadIdx.x & 63) * 16;
  asm volatile(
  "s_nop 4\n"
  "v_lshrrev_b32 v243, 8, v240\n"
  "v_and_b32 v236, 0xf0, v240\n"
  "s_add_u32 m0, s45, 0\n"
  "s_add_u32 s68, s44, 0\n"
  "buffer_load_dwordx4 v240, s[36:39], s68 offen lds\n"
  "v_lshl_or_b32 v243, v243, 15, v236\n"
  "s_add_u32 s69, s46, 0\n"
  "buffer_load_dwordx4 v[0:3], v243, s[36:39], s69 offen\n"
  "s_add_u32 s69, s46, 0x4000\n"
  "buffer_load_dwordx4 v[4:7], v243, s[36:39], s69 offen\n"
  "s_add_u32 m0, s45, 0x2000\n"
  "s_add_u32 s68, s44, 0x2000\n"
  "buffer_load_dwordx4 v240, s[36:39], s68 offen lds\n"
  "s_add_u32 s69, s46, 0x20000\n"
  "buffer_load_dwordx4 v[8:11], v243, s[36:39], s69 offen\n"
  "s_add_u32 s69, s46, 0x24000\n"
  "buffer_load_dwordx4 v[12:15], v243, s[36:39], s69 offen\n"
  "v_lshrrev_b32 v236, 6, v240\n"
  "v_and_b32 v237, 48, v240\n"
  "v_lshl_or_b32 v236, v236, 14, v237\n"
  "v_cmp_gt_u32 vcc, 0x80, v240\n"
  "v_mov_b32 v237, 0x7fffff00\n"
  "s_add_u32 m0, s45, 0x4000\n"
  "s_add_u32 s68, s44, 0x4000\n"
  "buffer_load_dwordx4 v240, s[36:39], s68 offen lds\n"
  "v_cndmask_b32 v237, v237, v236, vcc\n"
  "buffer_load_dwordx4 v[224:227], v236, s[36:39], s47 offen\n"
  "s_add_u32 s69, s47, 0x40000\n"
  "buffer_load_dwordx4 v[228:231], v237, s[36:39], s69 offen\n"
  "s_add_u32 m0, s45, 0x6000\n"
  "s_add_u32 s68, s44, 0x6000\n"
  "buffer_load_dwordx4 v240, s[36:39], s68 offen lds\n"
  "s_add_u32 s69, s46, 0x40000\n"
  "buffer_load_dwordx4 v[16:19], v243, s[36:39], s69 offen\n"
  "s_add_u32 s69, s46, 0x44000\n"
  "buffer_load_dwordx4 v[20:23], v243, s[36:39], s69 offen\n"
  "s_add_u32 m0, s45, 0x24000\n"
  "v_add_u32 v241, 0x10000, v240\n"
  "buffer_load_dwordx4 v240, s[36:39], s51 offen lds\n"
  "v_add_u32 v242, 0x20000, v240\n"
  "s_mov_b32 s70, 0x20030\n"
  "s_lshr_b32 s71, s45, 2\n"
  "s_cmp_lt_u32 s45, 0x1000\n"
  "s_cbranch_scc1 .Lk34_np\n"
  "s_setprio 1\n"
  ".Lk34_np:\n"
  "s_waitcnt vmcnt(9)\n"
  "s_barrier\n"
  "ds_read_b128 v[208:211], v240 offset:0\n"
  "ds_read_b128 v[212:215], v240 offset:1024\n"
  "ds_read_b128 v[216:219], v240 offset:2048\n"
  "ds_read_b128 v[220:223], v240 offset:3072\n"
  "v_swap_b32 v2, v4\n"
  "v_swap_b32 v3, v5\n"
  "s_waitcnt lgkmcnt(3)\n"
  "s_nop 0\n"
  "v_mfma_f32_16x16x32_bf16 v[144:147], v[208:211], v[0:3], 0\n"
  "s_add_u32 s69, s46, 0x60000\n"
  "buffer_load_dwordx4 v[24:27], v243, s[36:39], s69 offen\n"
  "v_mfma_f32_16x16x32_bf16 v[148:151], v[208:211], v[4:7], 0\n"
  "ds_read_b128 v[208:211], v240 offset:4096\n"
  "s_waitcnt lgkmcnt(3)\n"
  "v_mfma_f32_16x16x32_bf16 v[152:155], v[212:215], v[0:3], 0\n"
  "s_add_u32 m0, s45, 0x8000\n"
  "s_add_u32 s68, s44, 0x8000\n"
  "buffer_load_dwordx4 v240, s[36:39], s68 offen lds\n"
  "v_mfma_f32_16x16x32_bf16 v[156:159], v[212:215], v[4:7], 0\n"
  "ds_read_b128 v[212:215], v240 offset:5120\n"
  "s_waitcnt lgkmcnt(3)\n"
  "v_mfma_f32_16x16x32_bf16 v[160:163], v[216:219], v[0:3], 0\n"
  "v_mfma_f32_16x16x32_bf16 v[164:167], v[216:219], v[4:7], 0\n"
  "ds_read_b128 v[216:219], v240 offset:6144\n"
  "s_waitcnt lgkmcnt(3)\n"
  "v_mfma_f32_16x16x32_bf16 v[168:171], v[220:223], v[0:3], 0\n"
  "v_mfma_f32_16x16x32_bf16 v[172:175], v[220:223], v[4:7], 0\n"
  "ds_read_b128 v[220:223], v240 offset:7168\n"
  "s_waitcnt lgkmcnt(3)\n"
  "v_mfma_f32_16x16x32_bf16 v[176:179], v[208:211], v[0:3], 0\n"
  "s_add_u32 s69, s46, 0x64000\n"
  "buffer_load_dwordx4 v[28:31], v243, s[36:39], s69 offen\n"
  "v_mfma_f32_16x16x32_bf16 v[180:183], v[208:211], v[4:7], 0\n"
  "ds_read_b128 v[208:211], v240 offset:8192\n"
  "s_waitcnt lgkmcnt(3)\n"
  "v_mfma_f32_16x16x32_bf16 v[184:187], v[212:215], v[0:3], 0\n"
  "s_add_u32 m0, s45, 0xa000\n"
  "s_add_u32 s68, s44, 0xa000\n"
  "buffer_load_dwordx4 v240, s[36:39], s68 offen lds\n"
  "v_mfma_f32_16x16x32_bf16 v[188:191], v[212:215], v[4:7], 0\n"
  "ds_read_b128 v[212:215], v240 offset:9216\n"
  "s_waitcnt lgkmcnt(3)\n"
  "v_mfma_f32_16x16x32_bf16 v[192:195], v[216:219], v[0:3], 0\n"
  "v_mov_b32 v232, 0\n"
  "v_mov_b32 v233, 0\n"
  "v_mfma_f32_16x16x32_bf16 v[196:199], v[216:219], v[4:7], 0\n"
  "ds_read_b128 v[216:219], v240 offset:10240\n"
  "s_waitcnt lgkmcnt(3)\n"
  "v_mfma_f32_16x16x32_bf16 v[200:203], v[220:223], v[0:3], 0\n"
  "s_waitcnt vmcnt(11)\n"
  "v_swap_b32 v10, v12\n"
  "v_swap_b32 v11, v13\n"
  "v_mfma_f32_16x16x32_bf16 v[204:207], v[220:223], v[4:7], 0\n"
  "ds_read_b128 v[220:223], v240 offset:11264\n"
  "s_add_u32 s69, s46, 0x80000\n"
  "buffer_load_dwordx4 v[32:35], v243, s[36:39], s69 offen\n"
  "v_mov_b32 v234, 0\n"
  "v_mov_b32 v235, 0\n"
  "s_waitcnt lgkmcnt(3)\n"
  "v_mfma_f32_16x16x32_bf16 v[144:147], v[208:211], v[8:11], v[144:147]\n"
  "s_waitcnt vmcnt(10)\n"
  "v_dot2c_f32_bf16 v232, 0x3f803f80, v224\n"
  "v_dot2c_f32_bf16 v233, v224, v224\n"
  "v_mfma_f32_16x16x32_bf16 v[148:151], v[208:211], v[12:15], v[148:151]\n"
  "ds_read_b128 v[208:211], v240 offset:12288\n"
  "s_waitcnt lgkmcnt(3)\n"
  "v_mfma_f32_16x16x32_bf16 v[152:155], v[212:215], v[8:11], v[152:155]\n"
  "s_add_u32 m0, s45, 0xc000\n"
  "s_add_u32 s68, s44, 0xc000\n"
  "buffer_load_dwordx4 v240, s[36:39], s68 offen lds\n"
  "v_mfma_f32_16x16x32_bf16 v[156:159], v[212:215], v[12:15], v[156:159]\n"
  "ds_read_b128 v[212:215], v240 offset:13312\n"
  "v_dot2c_f32_bf16 v234, 0x3f803f80, v226\n"
  "v_dot2c_f32_bf16 v235, v226, v226\n"
  "s_waitcnt lgkmcnt(3)\n"
  "v_mfma_f32_16x16x32_bf16 v[160:163], v[216:219], v[8:11], v[160:163]\n"
  "s_add_u32 s69, s46, 0x84000\n"
  "buffer_load_dwordx4 v[36:39], v243, s[36:39], s69 offen\n"
  "v_mfma_f32_16x16x32_bf16 v[164:167], v[216:219], v[12:15], v[164:167]\n"
  "ds_read_b128 v[216:219], v240 offset:14336\n"
  "v_dot2c_f32_bf16 v232, 0x3f803f80, v225\n"
  "v_dot2c_f32_bf16 v233, v225, v225\n"
  "s_waitcnt lgkmcnt(3)\n"
  "v_mfma_f32_16x16x32_bf16 v[168:171], v[220:223], v[8:11], v[168:171]\n"
  "v_dot2c_f32_bf16 v234, 0x3f803f80, v227\n"
  "v_dot2c_f32_bf16 v235, v227, v227\n"
  "v_mfma_f32_16x16x32_bf16 v[172:175], v[220:223], v[12:15], v[172:175]\n"
  "ds_read_b128 v[220:223], v240 offset:15360\n"
  "s_waitcnt vmcnt(10)\n"
  "s_barrier\n"
  "s_waitcnt lgkmcnt(3)\n"
  "v_mfma_f32_16x16x32_bf16 v[176:179], v[208:211], v[8:11], v[176:179]\n"
  "v_dot2c_f32_bf16 v232, 0x3f803f80, v228\n"
  "v_dot2c_f32_bf16 v233, v228, v228\n"
  "v_mfma_f32_16x16x32_bf16 v[180:183], v[208:211], v[12:15], v[180:183]\n"
  "ds_read_b128 v[208:211], v240 offset:16384\n"
  "s_waitcnt lgkmcnt(3)\n"
  "v_mfma_f32_16x16x32_bf16 v[184:187], v[212:215], v[8:11], v[184:187]\n"
  "s_add_u32 m0, s45, 0xe000\n"
  "s_add_u32 s68, s44, 0xe000\n"
  "buffer_load_dwordx4 v240, s[36:39], s68 offen lds\n"
  "v_mfma_f32_16x16x32_bf16 v[188:191], v[212:215], v[12:15], v[188:191]\n"
  "ds_read_b128 v[212:215], v240 offset:17408\n"
  "v_dot2c_f32_bf16 v234, 0x3f803f80, v230\n"
  "v_dot2c_f32_bf16 v235, v230, v230\n"
  "s_waitcnt lgkmcnt(3)\n"
  "v_mfma_f32_16x16x32_bf16 v[192:195], v[216:219], v[8:11], v[192:195]\n"
  "s_add_u32 s69, s46, 0xa0000\n"
  "buffer_load_dwordx4 v[40:43], v243, s[36:39], s69 offen\n"
  "v_mfma_f32_16x16x32_bf16 v[196:199], v[216:219], v[12:15], v[196:199]\n"
  "ds_read_b128 v[216:219], v240 offset:18432\n"
  "v_dot2c_f32_bf16 v232, 0x3f803f80, v229\n"
  "v_dot2c_f32_bf16 v233, v229, v229\n"
  "s_waitcnt lgkmcnt(3)\n"
  "v_mfma_f32_16x16x32_bf16 v[200:203], v[220:223], v[8:11], v[200:203]\n"
  "s_waitcnt vmcnt(10)\n"
  "v_swap_b32 v18, v20\n"
  "v_swap_b32 v19, v21\n"
  "v_mfma_f32_16x16x32_bf16 v[204:207], v[220:223], v[12:15], v[204:207]\n"
  "ds_read_b128 v[220:223], v240 offset:19456\n"
  "v_dot2c_f32_bf16 v234, 0x3f803f80, v231\n"
  "v_dot2c_f32_bf16 v235, v231, v231\n"
  "s_waitcnt lgkmcnt(3)\n"
  "v_mfma_f32_16x16x32_bf16 v[144:147], v[208:211], v[16:19], v[144:147]\n"
  "s_add_u32 s69, s46, 0xa4000\n"
  "buffer_load_dwordx4 v[44:47], v243, s[36:39], s69 offen\n"
  "v_mfma_f32_16x16x32_bf16 v[148:151], v[208:211], v[20:23], v[148:151]\n"
  "ds_read_b128 v[208:211], v240 offset:20480\n"
  "v_lshrrev_b32 v236, 2, v240\n"
  "v_and_b32 v236, 0xc0, v236\n"
  "s_waitcnt lgkmcnt(3)\n"
  "v_mfma_f32_16x16x32_bf16 v[152:155], v[212:215], v[16:19], v[152:155]\n"
  "s_add_u32 m0, s45, 0x10000\n"
  "s_add_u32 s68, s44, 0x10000\n"
  "buffer_load_dwordx4 v240, s[36:39], s68 offen lds\n"
  "v_mfma_f32_16x16x32_bf16 v[156:159], v[212:215], v[20:23], v[156:159]\n"
  "ds_read_b128 v[212:215], v240 offset:21504\n"
  "v_and_or_b32 v236, v242, s70, v236\n"
  "v_add_u32 v236, s71, v236\n"
  "s_waitcnt lgkmcnt(3)\n"
  "v_mfma_f32_16x16x32_bf16 v[160:163], v[216:219], v[16:19], v[160:163]\n"
  "v_add_f32_dpp v232, v232, v232 row_ror:4 row_mask:0xf bank_mask:0xf\n"
  "v_add_f32_dpp v233, v233, v233 row_ror:4 row_mask:0xf bank_mask:0xf\n"
  "v_mfma_f32_16x16x32_bf16 v[164:167], v[216:219], v[20:23], v[164:167]\n"
  "ds_read_b128 v[216:219], v240 offset:22528\n"
  "s_waitcnt lgkmcnt(3)\n"
  "v_mfma_f32_16x16x32_bf16 v[168:171], v[220:223], v[16:19], v[168:171]\n"
  "v_add_f32_dpp v234, v234, v234 row_ror:4 row_mask:0xf bank_mask:0xf\n"
  "v_add_f32_dpp v235, v235, v235 row_ror:4 row_mask:0xf bank_mask:0xf\n"
  "v_mfma_f32_16x16x32_bf16 v[172:175], v[220:223], v[20:23], v[172:175]\n"
  "ds_read_b128 v[220:223], v240 offset:23552\n"
  "s_waitcnt lgkmcnt(3)\n"
  "v_mfma_f32_16x16x32_bf16 v[176:179], v[208:211], v[16:19], v[176:179]\n"
  "s_add_u32 s69, s46, 0xc0000\n"
  "buffer_load_dwordx4 v[48:51], v243, s[36:39], s69 offen\n"
  "v_mfma_f32_16x16x32_bf16 v[180:183], v[208:211], v[20:23], v[180:183]\n"
  "ds_read_b128 v[208:211], v240 offset:24576\n"
  "v_add_f32_dpp v232, v232, v232 row_ror:8 row_mask:0xf bank_mask:0xf\n"
  "v_add_f32_dpp v233, v233, v233 row_ror:8 row_mask:0xf bank_mask:0xf\n"
  "s_waitcnt lgkmcnt(3)\n"
  "v_mfma_f32_16x16x32_bf16 v[184:187], v[212:215], v[16:19], v[184:187]\n"
  "v_add_f32_dpp v234, v234, v234 row_ror:8 row_mask:0xf bank_mask:0xf\n"
  "v_add_f32_dpp v235, v235, v235 row_ror:8 row_mask:0xf bank_mask:0xf\n"
  "v_mfma_f32_16x16x32_bf16 v[188:191], v[212:215], v[20:23], v[188:191]\n"
  "ds_read_b128 v[212:215], v240 offset:25600\n"
  "s_waitcnt lgkmcnt(3)\n"
  "v_mfma_f32_16x16x32_bf16 v[192:195], v[216:219], v[16:19], v[192:195]\n"
  "ds_write_b128 v236, v[232:235] offset:24576\n"
  "v_mfma_f32_16x16x32_bf16 v[196:199], v[216:219], v[20:23], v[196:199]\n"
  "ds_read_b128 v[216:219], v240 offset:26624\n"
  "s_waitcnt lgkmcnt(4)\n"
  "v_mfma_f32_16x16x32_bf16 v[200:203], v[220:223], v[16:19], v[200:203]\n"
  "s_waitcnt vmcnt(9)\n"
  "v_swap_b32 v26, v28\n"
  "v_swap_b32 v27, v29\n"
  "v_mfma_f32_16x16x32_bf16 v[204:207], v[220:223], v[20:23], v[204:207]\n"
  "ds_read_b128 v[220:223], v240 offset:27648\n"
  "s_add_u32 s69, s46, 0xc4000\n"
  "buffer_load_dwordx4 v[52:55], v243, s[36:39], s69 offen\n"
  "s_waitcnt lgkmcnt(4)\n"
  "v_mfma_f32_16x16x32_bf16 v[144:147], v[208:211], v[24:27], v[144:147]\n"
  "v_mfma_f32_16x16x32_bf16 v[148:151], v[208:211], v[28:31], v[148:151]\n"
  "ds_read_b128 v[208:211], v240 offset:28672\n"
  "s_waitcnt lgkmcnt(4)\n"
  "v_mfma_f32_16x16x32_bf16 v[152:155], v[212:215], v[24:27], v[152:155]\n"
  "s_add_u32 m0, s45, 0x12000\n"
  "s_add_u32 s68, s44, 0x12000\n"
  "buffer_load_dwordx4 v240, s[36:39], s68 offen lds\n"
  "v_mfma_f32_16x16x32_bf16 v[156:159], v[212:215], v[28:31], v[156:159]\n"
  "ds_read_b128 v[212:215], v240 offset:29696\n"
  "s_waitcnt lgkmcnt(3)\n"
  "v_mfma_f32_16x16x32_bf16 v[160:163], v[216:219], v[24:27], v[160:163]\n"
  "s_add_u32 s69, s46, 0xe0000\n"
  "buffer_load_dwordx4 v[56:59], v243, s[36:39], s69 offen\n"
  "v_mfma_f32_16x16x32_bf16 v[164:167], v[216:219], v[28:31], v[164:167]\n"
  "ds_read_b128 v[216:219], v240 offset:30720\n"
  "s_waitcnt lgkmcnt(3)\n"
  "v_mfma_f32_16x16x32_bf16 v[168:171], v[220:223], v[24:27], v[168:171]\n"
  "v_mfma_f32_16x16x32_bf16 v[172:175], v[220:223], v[28:31], v[172:175]\n"
  "ds_read_b128 v[220:223], v240 offset:31744\n"
  "s_waitcnt vmcnt(11)\n"
  "s_barrier\n"
  "s_waitcnt lgkmcnt(3)\n"
  "v_mfma_f32_16x16x32_bf16 v[176:179], v[208:211], v[24:27], v[176:179]\n"
  "s_cmp_eq_u32 s45, 0x1c00\n"
  "s_cbranch_scc1 .Lk34_stat\n"
  ".Lk34_stat_r:\n"
  "v_mfma_f32_16x16x32_bf16 v[180:183], v[208:211], v[28:31], v[180:183]\n"
  "ds_read_b128 v[208:211], v240 offset:32768\n"
  "s_waitcnt lgkmcnt(3)\n"
  "v_mfma_f32_16x16x32_bf16 v[184:187], v[212:215], v[24:27], v[184:187]\n"
  "v_mfma_f32_16x16x32_bf16 v[188:191], v[212:215], v[28:31], v[188:191]\n"
  "ds_read_b128 v[212:215], v240 offset:33792\n"
  "s_waitcnt lgkmcnt(3)\n"
  "v_mfma_f32_16x16x32_bf16 v[192:195], v[216:219], v[24:27], v[192:195]\n"
  "s_add_u32 s69, s46, 0xe4000\n"
  "buffer_load_dwordx4 v[60:63], v243, s[36:39], s69 offen\n"
  "v_mfma_f32_16x16x32_bf16 v[196:199], v[216:219], v[28:31], v[196:199]\n"
  "ds_read_b128 v[216:219], v240 offset:34816\n"
  "s_waitcnt lgkmcnt(3)\n"
  "v_mfma_f32_16x16x32_bf16 v[200:203], v[220:223], v[24:27], v[200:203]\n"
  "s_waitcnt vmcnt(9)\n"
  "v_swap_b32 v34, v36\n"
  "v_swap_b32 v35, v37\n"
  "v_mfma_f32_16x16x32_bf16 v[204:207], v[220:223], v[28:31], v[204:207]\n"
  "ds_read_b128 v[220:223], v240 offset:35840\n"
  "s_waitcnt lgkmcnt(3)\n"
  "v_mfma_f32_16x16x32_bf16 v[144:147], v[208:211], v[32:35], v[144:147]\n"
  "s_add_u32 s69, s46, 0x100000\n"
  "buffer_load_dwordx4 v[64:67], v243, s[36:39], s69 offen\n"
  "v_mfma_f32_16x16x32_bf16 v[148:151], v[208:211], v[36:39], v[148:151]\n"
  "ds_read_b128 v[208:211], v240 offset:36864\n"
  "s_waitcnt lgkmcnt(3)\n"
  "v_mfma_f32_16x16x32_bf16 v[152:155], v[212:215], v[32:35], v[152:155]\n"
  "s_add_u32 m0, s45, 0x14000\n"
  "s_add_u32 s68, s44, 0x14000\n"
  "buffer_load_dwordx4 v240, s[36:39], s68 offen lds\n"
  "v_mfma_f32_16x16x32_bf16 v[156:159], v[212:215], v[36:39], v[156:159]\n"
  "ds_read_b128 v[212:215], v240 offset:37888\n"
  "s_waitcnt lgkmcnt(3)\n"
  "v_mfma_f32_16x16x32_bf16 v[160:163], v[216:219], v[32:35], v[160:163]\n"
  "v_mfma_f32_16x16x32_bf16 v[164:167], v[216:219], v[36:39], v[164:167]\n"
  "ds_read_b128 v[216:219], v240 offset:38912\n"
  "s_waitcnt lgkmcnt(3)\n"
  "v_mfma_f32_16x16x32_bf16 v[168:171], v[220:223], v[32:35], v[168:171]\n"
  "v_mfma_f32_16x16x32_bf16 v[172:175], v[220:223], v[36:39], v[172:175]\n"
  "ds_read_b128 v[220:223], v240 offset:39936\n"
  "s_waitcnt lgkmcnt(3)\n"
  "v_mfma_f32_16x16x32_bf16 v[176:179], v[208:211], v[32:35], v[176:179]\n"
  "s_add_u32 s69, s46, 0x104000\n"
  "buffer_load_dwordx4 v[68:71], v243, s[36:39], s69 offen\n"
  "v_mfma_f32_16x16x32_bf16 v[180:183], v[208:211], v[36:39], v[180:183]\n"
  "ds_read_b128 v[208:211], v240 offset:40960\n"
  "s_waitcnt lgkmcnt(3)\n"
  "v_mfma_f32_16x16x32_bf16 v[184:187], v[212:215], v[32:35], v[184:187]\n"
  "v_mfma_f32_16x16x32_bf16 v[188:191], v[212:215], v[36:39], v[188:191]\n"
  "ds_read_b128 v[212:215], v240 offset:41984\n"
  "s_waitcnt lgkmcnt(3)\n"
  "v_mfma_f32_16x16x32_bf16 v[192:195], v[216:219], v[32:35], v[192:195]\n"
  "v_mfma_f32_16x16x32_bf16 v[196:199], v[216:219], v[36:39], v[196:199]\n"
  "ds_read_b128 v[216:219], v240 offset:43008\n"
  "s_waitcnt lgkmcnt(3)\n"
  "v_mfma_f32_16x16x32_bf16 v[200:203], v[220:223], v[32:35], v[200:203]\n"
  "s_waitcnt vmcnt(9)\n"
  "v_swap_b32 v42, v44\n"
  "v_swap_b32 v43, v45\n"
  "v_mfma_f32_16x16x32_bf16 v[204:207], v[220:223], v[36:39], v[204:207]\n"
  "ds_read_b128 v[220:223], v240 offset:44032\n"
  "s_add_u32 s69, s46, 0x120000\n"
  "buffer_load_dwordx4 v[72:75], v243, s[36:39], s69 offen\n"
  "s_waitcnt lgkmcnt(3)\n"
  "v_mfma_f32_16x16x32_bf16 v[144:147], v[208:211], v[40:43], v[144:147]\n"
  "v_mfma_f32_16x16x32_bf16 v[148:151], v[208:211], v[44:47], v[148:151]\n"
  "ds_read_b128 v[208:211], v240 offset:45056\n"
  "s_waitcnt lgkmcnt(3)\n"
  "v_mfma_f32_16x16x32_bf16 v[152:155], v[212:215], v[40:43], v[152:155]\n"
  "s_add_u32 m0, s45, 0x16000\n"
  "s_add_u32 s68, s44, 0x16000\n"
  "buffer_load_dwordx4 v240, s[36:39], s68 offen lds\n"
  "v_mfma_f32_16x16x32_bf16 v[156:159], v[212:215], v[44:47], v[156:159]\n"
  "ds_read_b128 v[212:215], v240 offset:46080\n"
  "s_waitcnt lgkmcnt(3)\n"
  "v_mfma_f32_16x16x32_bf16 v[160:163], v[216:219], v[40:43], v[160:163]\n"
  "s_add_u32 s69, s46, 0x124000\n"
  "buffer_load_dwordx4 v[76:79], v243, s[36:39], s69 offen\n"
  "v_mfma_f32_16x16x32_bf16 v[164:167], v[216:219], v[44:47], v[164:167]\n"
  "ds_read_b128 v[216:219], v240 offset:47104\n"
  "s_waitcnt lgkmcnt(3)\n"
  "v_mfma_f32_16x16x32_bf16 v[168:171], v[220:223], v[40:43], v[168:171]\n"
  "v_mfma_f32_16x16x32_bf16 v[172:175], v[220:223], v[44:47], v[172:175]\n"
  "ds_read_b128 v[220:223], v240 offset:48128\n"
  "s_barrier\n"
  "s_waitcnt lgkmcnt(3)\n"
  "v_mfma_f32_16x16x32_bf16 v[176:179], v[208:211], v[40:43], v[176:179]\n"
  "v_mfma_f32_16x16x32_bf16 v[180:183], v[208:211], v[44:47], v[180:183]\n"
  "ds_read_b128 v[208:211], v240 offset:49152\n"
  "s_waitcnt lgkmcnt(3)\n"
  "v_mfma_f32_16x16x32_bf16 v[184:187], v[212:215], v[40:43], v[184:187]\n"
  "v_mfma_f32_16x16x32_bf16 v[188:191], v[212:215], v[44:47], v[188:191]\n"
  "ds_read_b128 v[212:215], v240 offset:50176\n"
  "s_waitcnt lgkmcnt(3)\n"
  "v_mfma_f32_16x16x32_bf16 v[192:195], v[216:219], v[40:43], v[192:195]\n"
  "s_add_u32 s69, s46, 0x140000\n"
  "buffer_load_dwordx4 v[80:83], v243, s[36:39], s69 offen\n"
  "v_mfma_f32_16x16x32_bf16 v[196:199], v[216:219], v[44:47], v[196:199]\n"
  "ds_read_b128 v[216:219], v240 offset:51200\n"
  "v_add_u32 v244, s45, v242\n"
  "v_min_u32 v244, 0x208f0, v244\n"
  "s_waitcnt lgkmcnt(3)\n"
  "v_mfma_f32_16x16x32_bf16 v[200:203], v[220:223], v[40:43], v[200:203]\n"
  "s_waitcnt vmcnt(10)\n"
  "v_swap_b32 v50, v52\n"
  "v_swap_b32 v51, v53\n"
  "v_mfma_f32_16x16x32_bf16 v[204:207], v[220:223], v[44:47], v[204:207]\n"
  "ds_read_b128 v[220:223], v240 offset:52224\n"
  "ds_read_b128 v[228:231], v244 offset:16384\n"
  "ds_read_b128 v[232:235], v244 offset:18688\n"
  "s_waitcnt lgkmcnt(5)\n"
  "v_mfma_f32_16x16x32_bf16 v[144:147], v[208:211], v[48:51], v[144:147]\n"
  "s_add_u32 s69, s46, 0x144000\n"
  "buffer_load_dwordx4 v[84:87], v243, s[36:39], s69 offen\n"
  "v_mfma_f32_16x16x32_bf16 v[148:151], v[208:211], v[52:55], v[148:151]\n"
  "ds_read_b128 v[208:211], v240 offset:53248\n"
  "ds_read_b128 v[236:239], v244 offset:20992\n"
  "s_waitcnt lgkmcnt(6)\n"
  "v_mfma_f32_16x16x32_bf16 v[152:155], v[212:215], v[48:51], v[152:155]\n"
  "s_add_u32 m0, s45, 0x18000\n"
  "s_add_u32 s68, s44, 0x18000\n"
  "buffer_load_dwordx4 v240, s[36:39], s68 offen lds\n"
  "v_mfma_f32_16x16x32_bf16 v[156:159], v[212:215], v[52:55], v[156:159]\n"
  "ds_read_b128 v[212:215], v240 offset:54272\n"
  "s_waitcnt lgkmcnt(6)\n"
  "v_mfma_f32_16x16x32_bf16 v[160:163], v[216:219], v[48:51], v[160:163]\n"
  "v_mfma_f32_16x16x32_bf16 v[164:167], v[216:219], v[52:55], v[164:167]\n"
  "ds_read_b128 v[216:219], v240 offset:55296\n"
  "s_waitcnt lgkmcnt(6)\n"
  "v_mfma_f32_16x16x32_bf16 v[168:171], v[220:223], v[48:51], v[168:171]\n"
  "s_waitcnt lgkmcnt(4)\n"
  "v_add_f32 v228, v228, v232\n"
  "v_add_f32 v229, v229, v233\n"
  "v_mfma_f32_16x16x32_bf16 v[172:175], v[220:223], v[52:55], v[172:175]\n"
  "ds_read_b128 v[220:223], v240 offset:56320\n"
  "s_waitcnt lgkmcnt(4)\n"
  "v_mfma_f32_16x16x32_bf16 v[176:179], v[208:211], v[48:51], v[176:179]\n"
  "s_add_u32 s69, s46, 0x160000\n"
  "buffer_load_dwordx4 v[88:91], v243, s[36:39], s69 offen\n"
  "v_mfma_f32_16x16x32_bf16 v[180:183], v[208:211], v[52:55], v[180:183]\n"
  "ds_read_b128 v[208:211], v240 offset:57344\n"
  "v_add_f32 v230, v230, v234\n"
  "v_add_f32 v231, v231, v235\n"
  "s_waitcnt lgkmcnt(3)\n"
  "v_mfma_f32_16x16x32_bf16 v[184:187], v[212:215], v[48:51], v[184:187]\n"
  "v_add_f32 v228, v228, v236\n"
  "v_add_f32 v229, v229, v237\n"
  "v_mfma_f32_16x16x32_bf16 v[188:191], v[212:215], v[52:55], v[188:191]\n"
  "ds_read_b128 v[212:215], v240 offset:58368\n"
  "s_waitcnt lgkmcnt(3)\n"
  "v_mfma_f32_16x16x32_bf16 v[192:195], v[216:219], v[48:51], v[192:195]\n"
  "v_add_f32 v230, v230, v238\n"
  "v_add_f32 v231, v231, v239\n"
  "v_mfma_f32_16x16x32_bf16 v[196:199], v[216:219], v[52:55], v[196:199]\n"
  "ds_read_b128 v[216:219], v240 offset:59392\n"
  "s_waitcnt lgkmcnt(3)\n"
  "v_mfma_f32_16x16x32_bf16 v[200:203], v[220:223], v[48:51], v[200:203]\n"
  "s_waitcnt vmcnt(10)\n"
  "v_swap_b32 v58, v60\n"
  "v_swap_b32 v59, v61\n"
  "v_mfma_f32_16x16x32_bf16 v[204:207], v[220:223], v[52:55], v[204:207]\n"
  "ds_read_b128 v[220:223], v240 offset:60416\n"
  "s_add_u32 s69, s46, 0x164000\n"
  "buffer_load_dwordx4 v[92:95], v243, s[36:39], s69 offen\n"
  "ds_write_b128 v244, v[228:231] offset:26624\n"
  "s_waitcnt lgkmcnt(4)\n"
  "v_mfma_f32_16x16x32_bf16 v[144:147], v[208:211], v[56:59], v[144:147]\n"
  "v_mfma_f32_16x16x32_bf16 v[148:151], v[208:211], v[60:63], v[148:151]\n"
  "ds_read_b128 v[208:211], v240 offset:61440\n"
  "s_waitcnt lgkmcnt(4)\n"
  "v_mfma_f32_16x16x32_bf16 v[152:155], v[212:215], v[56:59], v[152:155]\n"
  "s_add_u32 m0, s45, 0x1a000\n"
  "s_add_u32 s68, s44, 0x1a000\n"
  "buffer_load_dwordx4 v240, s[36:39], s68 offen lds\n"
  "v_mfma_f32_16x16x32_bf16 v[156:159], v[212:215], v[60:63], v[156:159]\n"
  "ds_read_b128 v[212:215], v240 offset:62464\n"
  "s_waitcnt lgkmcnt(4)\n"
  "v_mfma_f32_16x16x32_bf16 v[160:163], v[216:219], v[56:59], v[160:163]\n"
  "s_add_u32 s69, s46, 0x180000\n"
  "buffer_load_dwordx4 v[96:99], v243, s[36:39], s69 offen\n"
  "v_mfma_f32_16x16x32_bf16 v[164:167], v[216:219], v[60:63], v[164:167]\n"
  "ds_read_b128 v[216:219], v240 offset:63488\n"
  "s_waitcnt lgkmcnt(4)\n"
  "v_mfma_f32_16x16x32_bf16 v[168:171], v[220:223], v[56:59], v[168:171]\n"
  "v_mfma_f32_16x16x32_bf16 v[172:175], v[220:223], v[60:63], v[172:175]\n"
  "ds_read_b128 v[220:223], v240 offset:64512\n"
  "s_barrier\n"
  "s_waitcnt lgkmcnt(3)\n"
  "v_mfma_f32_16x16x32_bf16 v[176:179], v[208:211], v[56:59], v[176:179]\n"
  "v_and_b32 v245, 0xf0, v240\n"
  "v_mfma_f32_16x16x32_bf16 v[180:183], v[208:211], v[60:63], v[180:183]\n"
  "ds_read_b128 v[208:211], v241 offset:0\n"
  "s_waitcnt lgkmcnt(3)\n"
  "v_mfma_f32_16x16x32_bf16 v[184:187], v[212:215], v[56:59], v[184:187]\n"
  "v_mfma_f32_16x16x32_bf16 v[188:191], v[212:215], v[60:63], v[188:191]\n"
  "ds_read_b128 v[212:215], v241 offset:1024\n"
  "s_waitcnt lgkmcnt(3)\n"
  "v_mfma_f32_16x16x32_bf16 v[192:195], v[216:219], v[56:59], v[192:195]\n"
  "s_add_u32 s69, s46, 0x184000\n"
  "buffer_load_dwordx4 v[100:103], v243, s[36:39], s69 offen\n"
  "v_mfma_f32_16x16x32_bf16 v[196:199], v[216:219], v[60:63], v[196:199]\n"
  "ds_read_b128 v[216:219], v241 offset:2048\n"
  "v_lshrrev_b32 v244, 2, v240\n"
  "s_waitcnt lgkmcnt(3)\n"
  "v_mfma_f32_16x16x32_bf16 v[200:203], v[220:223], v[56:59], v[200:203]\n"
  "s_waitcnt vmcnt(11)\n"
  "v_swap_b32 v66, v68\n"
  "v_swap_b32 v67, v69\n"
  "v_mfma_f32_16x16x32_bf16 v[204:207], v[220:223], v[60:63], v[204:207]\n"
  "ds_read_b128 v[220:223], v241 offset:3072\n"
  "s_waitcnt lgkmcnt(3)\n"
  "v_mfma_f32_16x16x32_bf16 v[144:147], v[208:211], v[64:67], v[144:147]\n"
  "s_add_u32 s69, s46, 0x1a0000\n"
  "buffer_load_dwordx4 v[104:107], v243, s[36:39], s69 offen\n"
  "v_mfma_f32_16x16x32_bf16 v[148:151], v[208:211], v[68:71], v[148:151]\n"
  "ds_read_b128 v[208:211], v241 offset:4096\n"
  "v_and_b32 v250, 12, v244\n"
  "s_waitcnt lgkmcnt(3)\n"
  "v_mfma_f32_16x16x32_bf16 v[152:155], v[212:215], v[64:67], v[152:155]\n"
  "s_add_u32 m0, s45, 0x1c000\n"
  "s_add_u32 s68, s44, 0x1c000\n"
  "buffer_load_dwordx4 v240, s[36:39], s68 offen lds\n"
  "v_mfma_f32_16x16x32_bf16 v[156:159], v[212:215], v[68:71], v[156:159]\n"
  "ds_read_b128 v[212:215], v241 offset:5120\n"
  "s_waitcnt lgkmcnt(3)\n"
  "v_mfma_f32_16x16x32_bf16 v[160:163], v[216:219], v[64:67], v[160:163]\n"
  "v_and_b32 v244, 0xc0, v244\n"
  "v_mfma_f32_16x16x32_bf16 v[164:167], v[216:219], v[68:71], v[164:167]\n"
  "ds_read_b128 v[216:219], v241 offset:6144\n"
  "s_waitcnt lgkmcnt(3)\n"
  "v_mfma_f32_16x16x32_bf16 v[168:171], v[220:223], v[64:67], v[168:171]\n"
  "v_mfma_f32_16x16x32_bf16 v[172:175], v[220:223], v[68:71], v[172:175]\n"
  "ds_read_b128 v[220:223], v241 offset:7168\n"
  "s_waitcnt lgkmcnt(3)\n"
  "v_mfma_f32_16x16x32_bf16 v[176:179], v[208:211], v[64:67], v[176:179]\n"
  "s_add_u32 s69, s46, 0x1a4000\n"
  "buffer_load_dwordx4 v[108:111], v243, s[36:39], s69 offen\n"
  "v_mfma_f32_16x16x32_bf16 v[180:183], v[208:211], v[68:71], v[180:183]\n"
  "ds_read_b128 v[208:211], v241 offset:8192\n"
  "v_add_u32 v244, 0x26800, v244\n"
  "s_waitcnt lgkmcnt(3)\n"
  "v_mfma_f32_16x16x32_bf16 v[184:187], v[212:215], v[64:67], v[184:187]\n"
  "v_mfma_f32_16x16x32_bf16 v[188:191], v[212:215], v[68:71], v[188:191]\n"
  "ds_read_b128 v[212:215], v241 offset:9216\n"
  "s_waitcnt lgkmcnt(3)\n"
  "v_mfma_f32_16x16x32_bf16 v[192:195], v[216:219], v[64:67], v[192:195]\n"
  "v_lshlrev_b32 v246, 3, v240\n"
  "v_mfma_f32_16x16x32_bf16 v[196:199], v[216:219], v[68:71], v[196:199]\n"
  "ds_read_b128 v[216:219], v241 offset:10240\n"
  "s_waitcnt lgkmcnt(3)\n"
  "v_mfma_f32_16x16x32_bf16 v[200:203], v[220:223], v[64:67], v[200:203]\n"
  "s_waitcnt vmcnt(11)\n"
  "v_swap_b32 v74, v76\n"
  "v_swap_b32 v75, v77\n"
  "v_mfma_f32_16x16x32_bf16 v[204:207], v[220:223], v[68:71], v[204:207]\n"
  "ds_read_b128 v[220:223], v241 offset:11264\n"
  "s_add_u32 s69, s46, 0x1c0000\n"
  "buffer_load_dwordx4 v[112:115], v243, s[36:39], s69 offen\n"
  "v_and_b32 v246, 0x380, v246\n"
  "s_waitcnt lgkmcnt(3)\n"
  "v_mfma_f32_16x16x32_bf16 v[144:147], v[208:211], v[72:75], v[144:147]\n"
  "v_lshrrev_b32 v247, 1, v240\n"
  "v_mfma_f32_16x16x32_bf16 v[148:151], v[208:211], v[76:79], v[148:151]\n"
  "ds_read_b128 v[208:211], v241 offset:12288\n"
  "s_waitcnt lgkmcnt(3)\n"
  "v_mfma_f32_16x16x32_bf16 v[152:155], v[212:215], v[72:75], v[152:155]\n"
  "s_add_u32 m0, s45, 0x1e000\n"
  "s_add_u32 s68, s44, 0x1e000\n"
  "buffer_load_dwordx4 v240, s[36:39], s68 offen lds\n"
  "v_mfma_f32_16x16x32_bf16 v[156:159], v[212:215], v[76:79], v[156:159]\n"
  "ds_read_b128 v[212:215], v241 offset:13312\n"
  "v_and_b32 v247, 64, v247\n"
  "s_waitcnt lgkmcnt(3)\n"
  "v_mfma_f32_16x16x32_bf16 v[160:163], v[216:219], v[72:75], v[160:163]\n"
  "s_add_u32 s69, s46, 0x1c4000\n"
  "buffer_load_dwordx4 v[116:119], v243, s[36:39], s69 offen\n"
  "v_mfma_f32_16x16x32_bf16 v[164:167], v[216:219], v[76:79], v[164:167]\n"
  "ds_read_b128 v[216:219], v241 offset:14336\n"
  "v_lshrrev_b32 v248, 4, v240\n"
  "s_waitcnt lgkmcnt(3)\n"
  "v_mfma_f32_16x16x32_bf16 v[168:171], v[220:223], v[72:75], v[168:171]\n"
  "v_xor_b32 v248, v248, v240\n"
  "v_mfma_f32_16x16x32_bf16 v[172:175], v[220:223], v[76:79], v[172:175]\n"
  "ds_read_b128 v[220:223], v241 offset:15360\n"
  "s_barrier\n"
  "s_waitcnt lgkmcnt(3)\n"
  "v_mfma_f32_16x16x32_bf16 v[176:179], v[208:211], v[72:75], v[176:179]\n"
  "v_and_b32 v248, 32, v248\n"
  "v_mfma_f32_16x16x32_bf16 v[180:183], v[208:211], v[76:79], v[180:183]\n"
  "ds_read_b128 v[208:211], v241 offset:16384\n"
  "s_waitcnt lgkmcnt(3)\n"
  "v_mfma_f32_16x16x32_bf16 v[184:187], v[212:215], v[72:75], v[184:187]\n"
  "v_or3_b32 v246, v246, v247, v248\n"
  "v_mfma_f32_16x16x32_bf16 v[188:191], v[212:215], v[76:79], v[188:191]\n"
  "ds_read_b128 v[212:215], v241 offset:17408\n"
  "s_waitcnt lgkmcnt(3)\n"
  "v_mfma_f32_16x16x32_bf16 v[192:195], v[216:219], v[72:75], v[192:195]\n"
  "s_add_u32 s69, s46, 0x1e0000\n"
  "buffer_load_dwordx4 v[120:123], v243, s[36:39], s69 offen\n"
  "v_mfma_f32_16x16x32_bf16 v[196:199], v[216:219], v[76:79], v[196:199]\n"
  "ds_read_b128 v[216:219], v241 offset:18432\n"
  "v_lshrrev_b32 v248, 4, v240\n"
  "s_waitcnt lgkmcnt(3)\n"
  "v_mfma_f32_16x16x32_bf16 v[200:203], v[220:223], v[72:75], v[200:203]\n"
  "s_waitcnt vmcnt(13)\n"
  "v_swap_b32 v82, v84\n"
  "v_swap_b32 v83, v85\n"
  "v_mfma_f32_16x16x32_bf16 v[204:207], v[220:223], v[76:79], v[204:207]\n"
  "ds_read_b128 v[220:223], v241 offset:19456\n"
  "v_and_b32 v248, 16, v248\n"
  "s_waitcnt lgkmcnt(3)\n"
  "v_mfma_f32_16x16x32_bf16 v[144:147], v[208:211], v[80:83], v[144:147]\n"
  "s_add_u32 s69, s46, 0x1e4000\n"
  "buffer_load_dwordx4 v[124:127], v243, s[36:39], s69 offen\n"
  "v_mfma_f32_16x16x32_bf16 v[148:151], v[208:211], v[84:87], v[148:151]\n"
  "ds_read_b128 v[208:211], v241 offset:20480\n"
  "s_lshr_b32 s70, s45, 2\n"
  "s_and_b32 s70, s70, 0x400\n"
  "s_waitcnt lgkmcnt(3)\n"
  "v_mfma_f32_16x16x32_bf16 v[152:155], v[212:215], v[80:83], v[152:155]\n"
  "s_add_u32 m0, s45, 0x20000\n"
  "s_add_u32 s68, s44, 0x20000\n"
  "buffer_load_dwordx4 v240, s[36:39], s68 offen lds\n"
  "v_mfma_f32_16x16x32_bf16 v[156:159], v[212:215], v[84:87], v[156:159]\n"
  "ds_read_b128 v[212:215], v241 offset:21504\n"
  "v_or3_b32 v246, v246, v248, s70\n"
  "s_waitcnt lgkmcnt(3)\n"
  "v_mfma_f32_16x16x32_bf16 v[160:163], v[216:219], v[80:83], v[160:163]\n"
  "v_add_u32 v247, 0x10000, v246\n"
  "v_mfma_f32_16x16x32_bf16 v[164:167], v[216:219], v[84:87], v[164:167]\n"
  "ds_read_b128 v[216:219], v241 offset:22528\n"
  "s_waitcnt lgkmcnt(3)\n"
  "v_mfma_f32_16x16x32_bf16 v[168:171], v[220:223], v[80:83], v[168:171]\n"
  "v_add_u32 v248, 0x20000, v246\n"
  "v_mfma_f32_16x16x32_bf16 v[172:175], v[220:223], v[84:87], v[172:175]\n"
  "ds_read_b128 v[220:223], v241 offset:23552\n"
  "s_waitcnt lgkmcnt(3)\n"
  "v_mfma_f32_16x16x32_bf16 v[176:179], v[208:211], v[80:83], v[176:179]\n"
  "s_add_u32 s69, s46, 0x200000\n"
  "buffer_load_dwordx4 v[128:131], v243, s[36:39], s69 offen\n"
  "v_mfma_f32_16x16x32_bf16 v[180:183], v[208:211], v[84:87], v[180:183]\n"
  "ds_read_b128 v[208:211], v241 offset:24576\n"
  "s_waitcnt lgkmcnt(3)\n"
  "v_mfma_f32_16x16x32_bf16 v[184:187], v[212:215], v[80:83], v[184:187]\n"
  "v_mfma_f32_16x16x32_bf16 v[188:191], v[212:215], v[84:87], v[188:191]\n"
  "ds_read_b128 v[212:215], v241 offset:25600\n"
  "s_waitcnt lgkmcnt(3)\n"
  "v_mfma_f32_16x16x32_bf16 v[192:195], v[216:219], v[80:83], v[192:195]\n"
  "v_mfma_f32_16x16x32_bf16 v[196:199], v[216:219], v[84:87], v[196:199]\n"
  "ds_read_b128 v[216:219], v241 offset:26624\n"
  "s_waitcnt lgkmcnt(3)\n"
  "v_mfma_f32_16x16x32_bf16 v[200:203], v[220:223], v[80:83], v[200:203]\n"
  "s_waitcnt vmcnt(13)\n"
  "v_swap_b32 v90, v92\n"
  "v_swap_b32 v91, v93\n"
  "v_mfma_f32_16x16x32_bf16 v[204:207], v[220:223], v[84:87], v[204:207]\n"
  "ds_read_b128 v[220:223], v241 offset:27648\n"
  "s_add_u32 s69, s46, 0x204000\n"
  "buffer_load_dwordx4 v[132:135], v243, s[36:39], s69 offen\n"
  "s_waitcnt lgkmcnt(3)\n"
  "v_mfma_f32_16x16x32_bf16 v[144:147], v[208:211], v[88:91], v[144:147]\n"
  "v_mfma_f32_16x16x32_bf16 v[148:151], v[208:211], v[92:95], v[148:151]\n"
  "ds_read_b128 v[208:211], v241 offset:28672\n"
  "s_waitcnt lgkmcnt(3)\n"
  "v_mfma_f32_16x16x32_bf16 v[152:155], v[212:215], v[88:91], v[152:155]\n"
  "s_add_u32 m0, s45, 0x22000\n"
  "s_add_u32 s68, s44, 0x22000\n"
  "buffer_load_dwordx4 v240, s[36:39], s68 offen lds\n"
  "v_mfma_f32_16x16x32_bf16 v[156:159], v[212:215], v[92:95], v[156:159]\n"
  "ds_read_b128 v[212:215], v241 offset:29696\n"
  "s_waitcnt lgkmcnt(3)\n"
  "v_mfma_f32_16x16x32_bf16 v[160:163], v[216:219], v[88:91], v[160:163]\n"
  "s_add_u32 s69, s46, 0x220000\n"
  "buffer_load_dwordx4 v[136:139], v243, s[36:39], s69 offen\n"
  "v_mfma_f32_16x16x32_bf16 v[164:167], v[216:219], v[92:95], v[164:167]\n"
  "ds_read_b128 v[216:219], v241 offset:30720\n"
  "v_mov_b32 v228, -1\n"
  "v_mov_b32 v229, -1\n"
  "v_mov_b32 v230, -1\n"
  "v_mov_b32 v231, -1\n"
  "s_waitcnt lgkmcnt(3)\n"
  "v_mfma_f32_16x16x32_bf16 v[168:171], v[220:223], v[88:91], v[168:171]\n"
  "v_mfma_f32_16x16x32_bf16 v[172:175], v[220:223], v[92:95], v[172:175]\n"
  "ds_read_b128 v[220:223], v241 offset:31744\n"
  "s_waitcnt vmcnt(15)\n"
  "s_barrier\n"
  "s_waitcnt lgkmcnt(3)\n"
  "v_mfma_f32_16x16x32_bf16 v[176:179], v[208:211], v[88:91], v[176:179]\n"
  "s_add_u32 s69, s61, 0\n"
  "buffer_store_dwordx4 v[228:231], v240, s[36:39], s69 offen sc1\n"
  "v_mfma_f32_16x16x32_bf16 v[180:183], v[208:211], v[92:95], v[180:183]\n"
  "ds_read_b128 v[208:211], v241 offset:32768\n"
  "s_waitcnt lgkmcnt(3)\n"
  "v_mfma_f32_16x16x32_bf16 v[184:187], v[212:215], v[88:91], v[184:187]\n"
  "v_mfma_f32_16x16x32_bf16 v[188:191], v[212:215], v[92:95], v[188:191]\n"
  "ds_read_b128 v[212:215], v241 offset:33792\n"
  "s_waitcnt lgkmcnt(3)\n"
  "v_mfma_f32_16x16x32_bf16 v[192:195], v[216:219], v[88:91], v[192:195]\n"
  "s_add_u32 s69, s46, 0x224000\n"
  "buffer_load_dwordx4 v[140:143], v243, s[36:39], s69 offen\n"
  "v_mfma_f32_16x16x32_bf16 v[196:199], v[216:219], v[92:95], v[196:199]\n"
  "ds_read_b128 v[216:219], v241 offset:34816\n"
  "s_waitcnt lgkmcnt(3)\n"
  "v_mfma_f32_16x16x32_bf16 v[200:203], v[220:223], v[88:91], v[200:203]\n"
  "s_waitcnt vmcnt(15)\n"
  "v_swap_b32 v98, v100\n"
  "v_swap_b32 v99, v101\n"
  "v_mfma_f32_16x16x32_bf16 v[204:207], v[220:223], v[92:95], v[204:207]\n"
  "ds_read_b128 v[220:223], v241 offset:35840\n"
  "s_waitcnt lgkmcnt(3)\n"
  "v_mfma_f32_16x16x32_bf16 v[144:147], v[208:211], v[96:99], v[144:147]\n"
  "s_add_u32 s69, s61, 0x2000\n"
  "buffer_store_dwordx4 v[228:231], v240, s[36:39], s69 offen sc1\n"
  "v_mfma_f32_16x16x32_bf16 v[148:151], v[208:211], v[100:103], v[148:151]\n"
  "ds_read_b128 v[208:211], v241 offset:36864\n"
  "s_waitcnt lgkmcnt(3)\n"
  "v_mfma_f32_16x16x32_bf16 v[152:155], v[212:215], v[96:99], v[152:155]\n"
  "s_add_u32 m0, s45, 0\n"
  "s_add_u32 s68, s44, 0x24000\n"
  "buffer_load_dwordx4 v240, s[36:39], s68 offen lds\n"
  "v_mfma_f32_16x16x32_bf16 v[156:159], v[212:215], v[100:103], v[156:159]\n"
  "ds_read_b128 v[212:215], v241 offset:37888\n"
  "s_waitcnt lgkmcnt(3)\n"
  "v_mfma_f32_16x16x32_bf16 v[160:163], v[216:219], v[96:99], v[160:163]\n"
  "v_mfma_f32_16x16x32_bf16 v[164:167], v[216:219], v[100:103], v[164:167]\n"
  "ds_read_b128 v[216:219], v241 offset:38912\n"
  "s_waitcnt lgkmcnt(3)\n"
  "v_mfma_f32_16x16x32_bf16 v[168:171], v[220:223], v[96:99], v[168:171]\n"
  "v_mfma_f32_16x16x32_bf16 v[172:175], v[220:223], v[100:103], v[172:175]\n"
  "ds_read_b128 v[220:223], v241 offset:39936\n"
  "s_waitcnt lgkmcnt(3)\n"
  "v_mfma_f32_16x16x32_bf16 v[176:179], v[208:211], v[96:99], v[176:179]\n"
  "s_add_u32 s69, s61, 0x4000\n"
  "buffer_store_dwordx4 v[228:231], v240, s[36:39], s69 offen sc1\n"
  "v_mfma_f32_16x16x32_bf16 v[180:183], v[208:211], v[100:103], v[180:183]\n"
  "ds_read_b128 v[208:211], v241 offset:40960\n"
  "s_waitcnt lgkmcnt(3)\n"
  "v_mfma_f32_16x16x32_bf16 v[184:187], v[212:215], v[96:99], v[184:187]\n"
  "s_add_u32 m0, s45, 0x2000\n"
  "s_add_u32 s68, s44, 0x26000\n"
  "buffer_load_dwordx4 v240, s[36:39], s68 offen lds\n"
  "v_mfma_f32_16x16x32_bf16 v[188:191], v[212:215], v[100:103], v[188:191]\n"
  "ds_read_b128 v[212:215], v241 offset:41984\n"
  "s_waitcnt lgkmcnt(3)\n"
  "v_mfma_f32_16x16x32_bf16 v[192:195], v[216:219], v[96:99], v[192:195]\n"
  "v_mfma_f32_16x16x32_bf16 v[196:199], v[216:219], v[100:103], v[196:199]\n"
  "ds_read_b128 v[216:219], v241 offset:43008\n"
  "s_waitcnt lgkmcnt(3)\n"
  "v_mfma_f32_16x16x32_bf16 v[200:203], v[220:223], v[96:99], v[200:203]\n"
  "s_waitcnt vmcnt(16)\n"
  "v_swap_b32 v106, v108\n"
  "v_swap_b32 v107, v109\n"
  "v_mfma_f32_16x16x32_bf16 v[204:207], v[220:223], v[100:103], v[204:207]\n"
  "ds_read_b128 v[220:223], v241 offset:44032\n"
  "s_waitcnt lgkmcnt(3)\n"
  "v_mfma_f32_16x16x32_bf16 v[144:147], v[208:211], v[104:107], v[144:147]\n"
  "s_add_u32 s69, s61, 0x6000\n"
  "buffer_store_dwordx4 v[228:231], v240, s[36:39], s69 offen sc1\n"
  "v_mfma_f32_16x16x32_bf16 v[148:151], v[208:211], v[108:111], v[148:151]\n"
  "ds_read_b128 v[208:211], v241 offset:45056\n"
  "s_waitcnt lgkmcnt(3)\n"
  "v_mfma_f32_16x16x32_bf16 v[152:155], v[212:215], v[104:107], v[152:155]\n"
  "s_add_u32 m0, s45, 0x4000\n"
  "s_add_u32 s68, s44, 0x28000\n"
  "buffer_load_dwordx4 v240, s[36:39], s68 offen lds\n"
  "v_mfma_f32_16x16x32_bf16 v[156:159], v[212:215], v[108:111], v[156:159]\n"
  "ds_read_b128 v[212:215], v241 offset:46080\n"
  "s_waitcnt lgkmcnt(3)\n"
  "v_mfma_f32_16x16x32_bf16 v[160:163], v[216:219], v[104:107], v[160:163]\n"
  "v_mfma_f32_16x16x32_bf16 v[164:167], v[216:219], v[108:111], v[164:167]\n"
  "ds_read_b128 v[216:219], v241 offset:47104\n"
  "s_waitcnt lgkmcnt(3)\n"
  "v_mfma_f32_16x16x32_bf16 v[168:171], v[220:223], v[104:107], v[168:171]\n"
  "v_mfma_f32_16x16x32_bf16 v[172:175], v[220:223], v[108:111], v[172:175]\n"
  "ds_read_b128 v[220:223], v241 offset:48128\n"
  "s_waitcnt vmcnt(16)\n"
  "s_barrier\n"
  "s_waitcnt lgkmcnt(3)\n"
  "v_mfma_f32_16x16x32_bf16 v[176:179], v[208:211], v[104:107], v[176:179]\n"
  "buffer_load_dwordx4 v[224:227], v245, s[36:39], s49 offen sc1\n"
  "v_mfma_f32_16x16x32_bf16 v[180:183], v[208:211], v[108:111], v[180:183]\n"
  "ds_read_b128 v[208:211], v241 offset:49152\n"
  "s_waitcnt lgkmcnt(3)\n"
  "v_mfma_f32_16x16x32_bf16 v[184:187], v[212:215], v[104:107], v[184:187]\n"
  "s_add_u32 m0, s45, 0x6000\n"
  "s_add_u32 s68, s44, 0x2a000\n"
  "buffer_load_dwordx4 v240, s[36:39], s68 offen lds\n"
  "v_mfma_f32_16x16x32_bf16 v[188:191], v[212:215], v[108:111], v[188:191]\n"
  "ds_read_b128 v[212:215], v241 offset:50176\n"
  "s_waitcnt lgkmcnt(3)\n"
  "v_mfma_f32_16x16x32_bf16 v[192:195], v[216:219], v[104:107], v[192:195]\n"
  "v_mfma_f32_16x16x32_bf16 v[196:199], v[216:219], v[108:111], v[196:199]\n"
  "ds_read_b128 v[216:219], v241 offset:51200\n"
  "s_waitcnt lgkmcnt(3)\n"
  "v_mfma_f32_16x16x32_bf16 v[200:203], v[220:223], v[104:107], v[200:203]\n"
  "s_waitcnt vmcnt(17)\n"
  "v_swap_b32 v114, v116\n"
  "v_swap_b32 v115, v117\n"
  "v_mfma_f32_16x16x32_bf16 v[204:207], v[220:223], v[108:111], v[204:207]\n"
  "ds_read_b128 v[220:223], v241 offset:52224\n"
  "s_waitcnt lgkmcnt(3)\n"
  "v_mfma_f32_16x16x32_bf16 v[144:147], v[208:211], v[112:115], v[144:147]\n"
  "v_mfma_f32_16x16x32_bf16 v[148:151], v[208:211], v[116:119], v[148:151]\n"
  "ds_read_b128 v[208:211], v241 offset:53248\n"
  "s_waitcnt lgkmcnt(3)\n"
  "v_mfma_f32_16x16x32_bf16 v[152:155], v[212:215], v[112:115], v[152:155]\n"
  "s_add_u32 m0, s45, 0x8000\n"
  "s_add_u32 s68, s44, 0x2c000\n"
  "buffer_load_dwordx4 v240, s[36:39], s68 offen lds\n"
  "v_mfma_f32_16x16x32_bf16 v[156:159], v[212:215], v[116:119], v[156:159]\n"
  "ds_read_b128 v[212:215], v241 offset:54272\n"
  "s_waitcnt lgkmcnt(3)\n"
  "v_mfma_f32_16x16x32_bf16 v[160:163], v[216:219], v[112:115], v[160:163]\n"
  "v_mfma_f32_16x16x32_bf16 v[164:167], v[216:219], v[116:119], v[164:167]\n"
  "ds_read_b128 v[216:219], v241 offset:55296\n"
  "s_waitcnt lgkmcnt(3)\n"
  "v_mfma_f32_16x16x32_bf16 v[168:171], v[220:223], v[112:115], v[168:171]\n"
  "v_mfma_f32_16x16x32_bf16 v[172:175], v[220:223], v[116:119], v[172:175]\n"
  "ds_read_b128 v[220:223], v241 offset:56320\n"
  "s_waitcnt lgkmcnt(3)\n"
  "v_mfma_f32_16x16x32_bf16 v[176:179], v[208:211], v[112:115], v[176:179]\n"
  "v_mfma_f32_16x16x32_bf16 v[180:183], v[208:211], v[116:119], v[180:183]\n"
  "ds_read_b128 v[208:211], v241 offset:57344\n"
  "s_waitcnt lgkmcnt(3)\n"
  "v_mfma_f32_16x16x32_bf16 v[184:187], v[212:215], v[112:115], v[184:187]\n"
  "s_add_u32 m0, s45, 0xa000\n"
  "s_add_u32 s68, s44, 0x2e000\n"
  "buffer_load_dwordx4 v240, s[36:39], s68 offen lds\n"
  "v_mfma_f32_16x16x32_bf16 v[188:191], v[212:215], v[116:119], v[188:191]\n"
  "ds_read_b128 v[212:215], v241 offset:58368\n"
  "s_waitcnt lgkmcnt(3)\n"
  "v_mfma_f32_16x16x32_bf16 v[192:195], v[216:219], v[112:115], v[192:195]\n"
  "v_mfma_f32_16x16x32_bf16 v[196:199], v[216:219], v[116:119], v[196:199]\n"
  "ds_read_b128 v[216:219], v241 offset:59392\n"
  "s_waitcnt lgkmcnt(3)\n"
  "v_mfma_f32_16x16x32_bf16 v[200:203], v[220:223], v[112:115], v[200:203]\n"
  "s_waitcnt vmcnt(17)\n"
  "v_swap_b32 v122, v124\n"
  "v_swap_b32 v123, v125\n"
  "v_mfma_f32_16x16x32_bf16 v[204:207], v[220:223], v[116:119], v[204:207]\n"
  "ds_read_b128 v[220:223], v241 offset:60416\n"
  "s_waitcnt lgkmcnt(3)\n"
  "v_mfma_f32_16x16x32_bf16 v[144:147], v[208:211], v[120:123], v[144:147]\n"
  "v_mfma_f32_16x16x32_bf16 v[148:151], v[208:211], v[124:127], v[148:151]\n"
  "ds_read_b128 v[208:211], v241 offset:61440\n"
  "s_waitcnt lgkmcnt(3)\n"
  "v_mfma_f32_16x16x32_bf16 v[152:155], v[212:215], v[120:123], v[152:155]\n"
  "s_waitcnt vmcnt(14)\n"
  "v_swap_b32 v130, v132\n"
  "v_swap_b32 v131, v133\n"
  "v_mfma_f32_16x16x32_bf16 v[156:159], v[212:215], v[124:127], v[156:159]\n"
  "ds_read_b128 v[212:215], v241 offset:62464\n"
  "s_add_u32 m0, s45, 0xc000\n"
  "s_add_u32 s68, s44, 0x30000\n"
  "buffer_load_dwordx4 v240, s[36:39], s68 offen lds\n"
  "s_waitcnt lgkmcnt(3)\n"
  "v_mfma_f32_16x16x32_bf16 v[144:147], v[216:219], v[128:131], v[144:147]\n"
  "v_mfma_f32_16x16x32_bf16 v[148:151], v[216:219], v[132:135], v[148:151]\n"
  "ds_read_b128 v[216:219], v241 offset:63488\n"
  "s_waitcnt lgkmcnt(3)\n"
  "v_mfma_f32_16x16x32_bf16 v[152:155], v[220:223], v[128:131], v[152:155]\n"
  "s_waitcnt vmcnt(11)\n"
  "v_swap_b32 v138, v140\n"
  "v_swap_b32 v139, v141\n"
  "v_mfma_f32_16x16x32_bf16 v[156:159], v[220:223], v[132:135], v[156:159]\n"
  "ds_read_b128 v[220:223], v241 offset:64512\n"
  "s_waitcnt vmcnt(7)\n"
  "s_barrier\n"
  "s_waitcnt lgkmcnt(3)\n"
  "v_mfma_f32_16x16x32_bf16 v[144:147], v[208:211], v[136:139], v[144:147]\n"
  "ds_read_b128 v[228:231], v244 offset:0\n"
  "v_mfma_f32_16x16x32_bf16 v[148:151], v[208:211], v[140:143], v[148:151]\n"
  "ds_read_b128 v[208:211], v242 offset:0\n"
  "s_waitcnt lgkmcnt(4)\n"
  "v_mfma_f32_16x16x32_bf16 v[152:155], v[212:215], v[136:139], v[152:155]\n"
  "s_add_u32 m0, s45, 0xe000\n"
  "s_add_u32 s68, s44, 0x32000\n"
  "buffer_load_dwordx4 v240, s[36:39], s68 offen lds\n"
  "v_mfma_f32_16x16x32_bf16 v[156:159], v[212:215], v[140:143], v[156:159]\n"
  "ds_read_b128 v[212:215], v242 offset:1024\n"
  "s_waitcnt vmcnt(5)\n"
  "v_max3_u32 v236, v224, v225, v226\n"
  "v_max_u32 v236, v236, v227\n"
  "v_cmp_eq_u32 vcc, -1, v236\n"
  "s_cbranch_vccnz .Lk34_stp\n"
  ".Lk34_stp_r:\n"
  "ds_read_b128 v[232:235], v244 offset:16\n"
  "s_waitcnt lgkmcnt(5)\n"
  "v_mfma_f32_16x16x32_bf16 v[160:163], v[216:219], v[120:123], v[160:163]\n"
  "v_mfma_f32_16x16x32_bf16 v[164:167], v[216:219], v[124:127], v[164:167]\n"
  "ds_read_b128 v[216:219], v242 offset:2048\n"
  "s_waitcnt lgkmcnt(5)\n"
  "v_mfma_f32_16x16x32_bf16 v[168:171], v[220:223], v[120:123], v[168:171]\n"
  "s_waitcnt lgkmcnt(4)\n"
  "v_fma_f32 v236, v225, v228, v229\n"
  "v_fma_f32 v237, v225, v230, v231\n"
  "v_mfma_f32_16x16x32_bf16 v[172:175], v[220:223], v[124:127], v[172:175]\n"
  "ds_read_b128 v[220:223], v242 offset:3072\n"
  "v_fmac_f32 v229, v227, v228\n"
  "s_waitcnt lgkmcnt(4)\n"
  "v_mfma_f32_16x16x32_bf16 v[176:179], v[208:211], v[120:123], v[176:179]\n"
  "v_fmac_f32 v231, v227, v230\n"
  "v_fmac_f32 v236, v224, v144\n"
  "v_mfma_f32_16x16x32_bf16 v[180:183], v[208:211], v[124:127], v[180:183]\n"
  "ds_read_b128 v[208:211], v242 offset:4096\n"
  "v_fmac_f32 v237, v224, v152\n"
  "s_waitcnt lgkmcnt(4)\n"
  "v_mfma_f32_16x16x32_bf16 v[184:187], v[212:215], v[120:123], v[184:187]\n"
  "s_add_u32 m0, s45, 0x10000\n"
  "s_add_u32 s68, s44, 0x34000\n"
  "buffer_load_dwordx4 v240, s[36:39], s68 offen lds\n"
  "v_fmac_f32 v229, v226, v148\n"
  "v_mfma_f32_16x16x32_bf16 v[188:191], v[212:215], v[124:127], v[188:191]\n"
  "ds_read_b128 v[212:215], v242 offset:5120\n"
  "v_fmac_f32 v231, v226, v156\n"
  "v_mul_f32 v144, v236, v237\n"
  "s_waitcnt lgkmcnt(3)\n"
  "v_mfma_f32_16x16x32_bf16 v[192:195], v[216:219], v[120:123], v[192:195]\n"
  "v_mul_f32 v148, v229, v231\n"
  "ds_read_b128 v[228:231], v244 offset:32\n"
  "v_fma_f32 v236, v225, v232, v233\n"
  "v_mfma_f32_16x16x32_bf16 v[196:199], v[216:219], v[124:127], v[196:199]\n"
  "ds_read_b128 v[216:219], v242 offset:6144\n"
  "v_fma_f32 v237, v225, v234, v235\n"
  "s_waitcnt lgkmcnt(4)\n"
  "v_mfma_f32_16x16x32_bf16 v[200:203], v[220:223], v[120:123], v[200:203]\n"
  "v_fmac_f32 v233, v227, v232\n"
  "v_fmac_f32 v235, v227, v234\n"
  "v_mfma_f32_16x16x32_bf16 v[204:207], v[220:223], v[124:127], v[204:207]\n"
  "ds_read_b128 v[220:223], v242 offset:7168\n"
  "v_fmac_f32 v236, v224, v145\n"
  "s_waitcnt lgkmcnt(4)\n"
  "v_mfma_f32_16x16x32_bf16 v[160:163], v[208:211], v[128:131], v[160:163]\n"
  "v_fmac_f32 v237, v224, v153\n"
  "v_fmac_f32 v233, v226, v149\n"
  "v_mfma_f32_16x16x32_bf16 v[164:167], v[208:211], v[132:135], v[164:167]\n"
  "ds_read_b128 v[208:211], v242 offset:8192\n"
  "v_fmac_f32 v235, v226, v157\n"
  "s_waitcnt lgkmcnt(4)\n"
  "v_mfma_f32_16x16x32_bf16 v[168:171], v[212:215], v[128:131], v[168:171]\n"
  "s_add_u32 m0, s45, 0x12000\n"
  "s_add_u32 s68, s44, 0x36000\n"
  "buffer_load_dwordx4 v240, s[36:39], s68 offen lds\n"
  "v_mul_f32 v145, v236, v237\n"
  "v_mfma_f32_16x16x32_bf16 v[172:175], v[212:215], v[132:135], v[172:175]\n"
  "ds_read_b128 v[212:215], v242 offset:9216\n"
  "v_mul_f32 v149, v233, v235\n"
  "ds_read_b128 v[232:235], v244 offset:48\n"
  "s_waitcnt lgkmcnt(5)\n"
  "v_fma_f32 v236, v225, v228, v229\n"
  "s_waitcnt lgkmcnt(4)\n"
  "v_mfma_f32_16x16x32_bf16 v[176:179], v[216:219], v[128:131], v[176:179]\n"
  "v_fma_f32 v237, v225, v230, v231\n"
  "v_fmac_f32 v229, v227, v228\n"
  "v_mfma_f32_16x16x32_bf16 v[180:183], v[216:219], v[132:135], v[180:183]\n"
  "ds_read_b128 v[216:219], v242 offset:10240\n"
  "v_fmac_f32 v231, v227, v230\n"
  "s_waitcnt lgkmcnt(4)\n"
  "v_mfma_f32_16x16x32_bf16 v[184:187], v[220:223], v[128:131], v[184:187]\n"
  "v_fmac_f32 v236, v224, v146\n"
  "v_fmac_f32 v237, v224, v154\n"
  "v_mfma_f32_16x16x32_bf16 v[188:191], v[220:223], v[132:135], v[188:191]\n"
  "ds_read_b128 v[220:223], v242 offset:11264\n"
  "v_fmac_f32 v229, v226, v150\n"
  "s_waitcnt lgkmcnt(4)\n"
  "v_mfma_f32_16x16x32_bf16 v[192:195], v[208:211], v[128:131], v[192:195]\n"
  "v_fmac_f32 v231, v226, v158\n"
  "v_mul_f32 v146, v236, v237\n"
  "v_mfma_f32_16x16x32_bf16 v[196:199], v[208:211], v[132:135], v[196:199]\n"
  "ds_read_b128 v[208:211], v242 offset:12288\n"
  "v_mul_f32 v150, v229, v231\n"
  "s_waitcnt lgkmcnt(4)\n"
  "v_mfma_f32_16x16x32_bf16 v[200:203], v[212:215], v[128:131], v[200:203]\n"
  "s_add_u32 m0, s45, 0x14000\n"
  "s_add_u32 s68, s44, 0x38000\n"
  "buffer_load_dwordx4 v240, s[36:39], s68 offen lds\n"
  "s_waitcnt lgkmcnt(3)\n"
  "v_fma_f32 v236, v225, v232, v233\n"
  "v_mfma_f32_16x16x32_bf16 v[204:207], v[212:215], v[132:135], v[204:207]\n"
  "ds_read_b128 v[212:215], v242 offset:13312\n"
  "v_fma_f32 v237, v225, v234, v235\n"
  "v_fmac_f32 v233, v227, v232\n"
  "s_waitcnt lgkmcnt(3)\n"
  "v_mfma_f32_16x16x32_bf16 v[160:163], v[216:219], v[136:139], v[160:163]\n"
  "v_fmac_f32 v235, v227, v234\n"
  "v_fmac_f32 v236, v224, v147\n"
  "v_mfma_f32_16x16x32_bf16 v[164:167], v[216:219], v[140:143], v[164:167]\n"
  "ds_read_b128 v[216:219], v242 offset:14336\n"
  "v_fmac_f32 v237, v224, v155\n"
  "s_waitcnt lgkmcnt(3)\n"
  "v_mfma_f32_16x16x32_bf16 v[168:171], v[220:223], v[136:139], v[168:171]\n"
  "v_fmac_f32 v233, v226, v151\n"
  "v_fmac_f32 v235, v226, v159\n"
  "v_mfma_f32_16x16x32_bf16 v[172:175], v[220:223], v[140:143], v[172:175]\n"
  "ds_read_b128 v[220:223], v242 offset:15360\n"
  "v_mul_f32 v147, v236, v237\n"
  "s_waitcnt lgkmcnt(3)\n"
  "v_mfma_f32_16x16x32_bf16 v[176:179], v[208:211], v[136:139], v[176:179]\n"
  "v_mul_f32 v151, v233, v235\n"
  "v_cvt_pk_bf16_f32 v152, v144, v145\n"
  "v_mfma_f32_16x16x32_bf16 v[180:183], v[208:211], v[140:143], v[180:183]\n"
  "ds_read_b128 v[208:211], v240 offset:0\n"
  "v_cvt_pk_bf16_f32 v153, v146, v147\n"
  "s_waitcnt lgkmcnt(3)\n"
  "v_mfma_f32_16x16x32_bf16 v[184:187], v[212:215], v[136:139], v[184:187]\n"
  "s_add_u32 m0, s45, 0x16000\n"
  "s_add_u32 s68, s44, 0x3a000\n"
  "buffer_load_dwordx4 v240, s[36:39], s68 offen lds\n"
  "v_cvt_pk_bf16_f32 v154, v148, v149\n"
  "v_mfma_f32_16x16x32_bf16 v[188:191], v[212:215], v[140:143], v[188:191]\n"
  "ds_read_b128 v[212:215], v240 offset:1024\n"
  "v_cvt_pk_bf16_f32 v155, v150, v151\n"
  "ds_read_b128 v[228:231], v244 offset:256\n"
  "s_waitcnt lgkmcnt(4)\n"
  "v_mfma_f32_16x16x32_bf16 v[192:195], v[216:219], v[136:139], v[192:195]\n"
  "s_add_u32 s69, s48, 0\n"
  "buffer_store_dwordx4 v[152:155], v240, s[36:39], s69 offen sc1\n"
  "v_mfma_f32_16x16x32_bf16 v[196:199], v[216:219], v[140:143], v[196:199]\n"
  "ds_read_b128 v[216:219], v240 offset:2048\n"
  "ds_read_b128 v[232:235], v244 offset:272\n"
  "s_waitcnt lgkmcnt(5)\n"
  "v_mfma_f32_16x16x32_bf16 v[200:203], v[220:223], v[136:139], v[200:203]\n"
  "v_mfma_f32_16x16x32_bf16 v[204:207], v[220:223], v[140:143], v[204:207]\n"
  "ds_read_b128 v[220:223], v240 offset:3072\n"
  "s_waitcnt lgkmcnt(5)\n"
  "v_mfma_f32_16x16x32_bf16 v[144:147], v[208:211], v[0:3], 0\n"
  "s_waitcnt lgkmcnt(3)\n"
  "v_fma_f32 v236, v225, v228, v229\n"
  "v_fma_f32 v237, v225, v230, v231\n"
  "v_mfma_f32_16x16x32_bf16 v[148:151], v[208:211], v[4:7], 0\n"
  "ds_read_b128 v[208:211], v240 offset:4096\n"
  "v_fmac_f32 v229, v227, v228\n"
  "v_mfma_f32_16x16x32_bf16 v[152:155], v[212:215], v[0:3], 0\n"
  "s_add_u32 m0, s45, 0x18000\n"
  "s_add_u32 s68, s44, 0x3c000\n"
  "buffer_load_dwordx4 v240, s[36:39], s68 offen lds\n"
  "v_fmac_f32 v231, v227, v230\n"
  "v_mfma_f32_16x16x32_bf16 v[156:159], v[212:215], v[4:7], 0\n"
  "ds_read_b128 v[212:215], v240 offset:5120\n"
  "v_fmac_f32 v236, v224, v160\n"
  "v_fmac_f32 v237, v224, v168\n"
  "s_waitcnt lgkmcnt(4)\n"
  "v_mfma_f32_16x16x32_bf16 v[144:147], v[216:219], v[8:11], v[144:147]\n"
  "v_fmac_f32 v229, v226, v164\n"
  "v_fmac_f32 v231, v226, v172\n"
  "v_mfma_f32_16x16x32_bf16 v[148:151], v[216:219], v[12:15], v[148:151]\n"
  "ds_read_b128 v[216:219], v240 offset:6144\n"
  "v_mul_f32 v160, v236, v237\n"
  "s_waitcnt lgkmcnt(3)\n"
  "v_mfma_f32_16x16x32_bf16 v[152:155], v[220:223], v[8:11], v[152:155]\n"
  "v_mul_f32 v164, v229, v231\n"
  "ds_read_b128 v[228:231], v244 offset:288\n"
  "v_fma_f32 v236, v225, v232, v233\n"
  "v_mfma_f32_16x16x32_bf16 v[156:159], v[220:223], v[12:15], v[156:159]\n"
  "ds_read_b128 v[220:223], v240 offset:7168\n"
  "v_fma_f32 v237, v225, v234, v235\n"
  "s_waitcnt lgkmcnt(4)\n"
  "v_mfma_f32_16x16x32_bf16 v[144:147], v[208:211], v[16:19], v[144:147]\n"
  "v_fmac_f32 v233, v227, v232\n"
  "v_fmac_f32 v235, v227, v234\n"
  "v_mfma_f32_16x16x32_bf16 v[148:151], v[208:211], v[20:23], v[148:151]\n"
  "ds_read_b128 v[208:211], v240 offset:8192\n"
  "v_fmac_f32 v236, v224, v161\n"
  "s_waitcnt lgkmcnt(4)\n"
  "v_mfma_f32_16x16x32_bf16 v[152:155], v[212:215], v[16:19], v[152:155]\n"
  "s_add_u32 m0, s45, 0x1a000\n"
  "s_add_u32 s68, s44, 0x3e000\n"
  "buffer_load_dwordx4 v240, s[36:39], s68 offen lds\n"
  "v_fmac_f32 v237, v224, v169\n"
  "v_mfma_f32_16x16x32_bf16 v[156:159], v[212:215], v[20:23], v[156:159]\n"
  "ds_read_b128 v[212:215], v240 offset:9216\n"
  "v_fmac_f32 v233, v226, v165\n"
  "v_fmac_f32 v235, v226, v173\n"
  "s_waitcnt lgkmcnt(4)\n"
  "v_mfma_f32_16x16x32_bf16 v[144:147], v[216:219], v[24:27], v[144:147]\n"
  "v_mul_f32 v161, v236, v237\n"
  "v_mul_f32 v165, v233, v235\n"
  "v_mfma_f32_16x16x32_bf16 v[148:151], v[216:219], v[28:31], v[148:151]\n"
  "ds_read_b128 v[216:219], v240 offset:10240\n"
  "ds_read_b128 v[232:235], v244 offset:304\n"
  "s_waitcnt lgkmcnt(5)\n"
  "v_fma_f32 v236, v225, v228, v229\n"
  "s_waitcnt lgkmcnt(4)\n"
  "v_mfma_f32_16x16x32_bf16 v[152:155], v[220:223], v[24:27], v[152:155]\n"
  "v_fma_f32 v237, v225, v230, v231\n"
  "v_fmac_f32 v229, v227, v228\n"
  "v_mfma_f32_16x16x32_bf16 v[156:159], v[220:223], v[28:31], v[156:159]\n"
  "ds_read_b128 v[220:223], v240 offset:11264\n"
  "v_fmac_f32 v231, v227, v230\n"
  "s_waitcnt lgkmcnt(4)\n"
  "v_mfma_f32_16x16x32_bf16 v[144:147], v[208:211], v[32:35], v[144:147]\n"
  "v_fmac_f32 v236, v224, v162\n"
  "v_fmac_f32 v237, v224, v170\n"
  "v_mfma_f32_16x16x32_bf16 v[148:151], v[208:211], v[36:39], v[148:151]\n"
  "ds_read_b128 v[208:211], v240 offset:12288\n"
  "v_fmac_f32 v229, v226, v166\n"
  "s_waitcnt lgkmcnt(4)\n"
  "v_mfma_f32_16x16x32_bf16 v[152:155], v[212:215], v[32:35], v[152:155]\n"
  "s_add_u32 m0, s45, 0x1c000\n"
  "s_add_u32 s68, s44, 0x40000\n"
  "buffer_load_dwordx4 v240, s[36:39], s68 offen lds\n"
  "v_fmac_f32 v231, v226, v174\n"
  "v_mfma_f32_16x16x32_bf16 v[156:159], v[212:215], v[36:39], v[156:159]\n"
  "ds_read_b128 v[212:215], v240 offset:13312\n"
  "v_mul_f32 v162, v236, v237\n"
  "v_mul_f32 v166, v229, v231\n"
  "s_waitcnt lgkmcnt(4)\n"
  "v_mfma_f32_16x16x32_bf16 v[144:147], v[216:219], v[40:43], v[144:147]\n"
  "s_waitcnt lgkmcnt(3)\n"
  "v_fma_f32 v236, v225, v232, v233\n"
  "v_fma_f32 v237, v225, v234, v235\n"
  "v_mfma_f32_16x16x32_bf16 v[148:151], v[216:219], v[44:47], v[148:151]\n"
  "ds_read_b128 v[216:219], v240 offset:14336\n"
  "v_fmac_f32 v233, v227, v232\n"
  "s_waitcnt lgkmcnt(3)\n"
  "v_mfma_f32_16x16x32_bf16 v[152:155], v[220:223], v[40:43], v[152:155]\n"
  "v_fmac_f32 v235, v227, v234\n"
  "v_fmac_f32 v236, v224, v163\n"
  "v_mfma_f32_16x16x32_bf16 v[156:159], v[220:223], v[44:47], v[156:159]\n"
  "ds_read_b128 v[220:223], v240 offset:15360\n"
  "v_fmac_f32 v237, v224, v171\n"
  "s_waitcnt vmcnt(10)\n"
  "s_barrier\n"
  "s_waitcnt lgkmcnt(3)\n"
  "v_mfma_f32_16x16x32_bf16 v[144:147], v[208:211], v[48:51], v[144:147]\n"
  "v_fmac_f32 v233, v226, v167\n"
  "v_fmac_f32 v235, v226, v175\n"
  "v_mfma_f32_16x16x32_bf16 v[148:151], v[208:211], v[52:55], v[148:151]\n"
  "ds_read_b128 v[208:211], v240 offset:16384\n"
  "v_mul_f32 v163, v236, v237\n"
  "s_waitcnt lgkmcnt(3)\n"
  "v_mfma_f32_16x16x32_bf16 v[152:155], v[212:215], v[48:51], v[152:155]\n"
  "s_add_u32 m0, s45, 0x1e000\n"
  "s_add_u32 s68, s44, 0x42000\n"
  "buffer_load_dwordx4 v240, s[36:39], s68 offen lds\n"
  "v_mul_f32 v167, v233, v235\n"
  "v_mfma_f32_16x16x32_bf16 v[156:159], v[212:215], v[52:55], v[156:159]\n"
  "ds_read_b128 v[212:215], v240 offset:17408\n"
  "v_cvt_pk_bf16_f32 v168, v160, v161\n"
  "v_cvt_pk_bf16_f32 v169, v162, v163\n"
  "s_waitcnt lgkmcnt(3)\n"
  "v_mfma_f32_16x16x32_bf16 v[144:147], v[216:219], v[56:59], v[144:147]\n"
  "v_cvt_pk_bf16_f32 v170, v164, v165\n"
  "v_cvt_pk_bf16_f32 v171, v166, v167\n"
  "v_mfma_f32_16x16x32_bf16 v[148:151], v[216:219], v[60:63], v[148:151]\n"
  "ds_read_b128 v[216:219], v240 offset:18432\n"
  "ds_read_b128 v[228:231], v244 offset:512\n"
  "s_waitcnt lgkmcnt(4)\n"
  "v_mfma_f32_16x16x32_bf16 v[152:155], v[220:223], v[56:59], v[152:155]\n"
  "s_add_u32 s69, s48, 0x2000\n"
  "buffer_store_dwordx4 v[168:171], v240, s[36:39], s69 offen sc1\n"
  "v_mfma_f32_16x16x32_bf16 v[156:159], v[220:223], v[60:63], v[156:159]\n"
  "ds_read_b128 v[220:223], v240 offset:19456\n"
  "ds_read_b128 v[232:235], v244 offset:528\n"
  "s_waitcnt lgkmcnt(5)\n"
  "v_mfma_f32_16x16x32_bf16 v[144:147], v[208:211], v[64:67], v[144:147]\n"
  "v_mfma_f32_16x16x32_bf16 v[148:151], v[208:211], v[68:71], v[148:151]\n"
  "ds_read_b128 v[208:211], v240 offset:20480\n"
  "s_waitcnt lgkmcnt(5)\n"
  "v_mfma_f32_16x16x32_bf16 v[152:155], v[212:215], v[64:67], v[152:155]\n"
  "s_add_u32 m0, s45, 0x20000\n"
  "s_add_u32 s68, s44, 0x44000\n"
  "buffer_load_dwordx4 v240, s[36:39], s68 offen lds\n"
  "s_waitcnt lgkmcnt(3)\n"
  "v_fma_f32 v236, v225, v228, v229\n"
  "v_mfma_f32_16x16x32_bf16 v[156:159], v[212:215], v[68:71], v[156:159]\n"
  "ds_read_b128 v[212:215], v240 offset:21504\n"
  "v_fma_f32 v237, v225, v230, v231\n"
  "v_fmac_f32 v229, v227, v228\n"
  "v_mfma_f32_16x16x32_bf16 v[144:147], v[216:219], v[72:75], v[144:147]\n"
  "v_fmac_f32 v231, v227, v230\n"
  "v_fmac_f32 v236, v224, v176\n"
  "v_mfma_f32_16x16x32_bf16 v[148:151], v[216:219], v[76:79], v[148:151]\n"
  "ds_read_b128 v[216:219], v240 offset:22528\n"
  "v_fmac_f32 v237, v224, v184\n"
  "s_waitcnt lgkmcnt(4)\n"
  "v_mfma_f32_16x16x32_bf16 v[152:155], v[220:223], v[72:75], v[152:155]\n"
  "v_fmac_f32 v229, v226, v180\n"
  "v_fmac_f32 v231, v226, v188\n"
  "v_mfma_f32_16x16x32_bf16 v[156:159], v[220:223], v[76:79], v[156:159]\n"
  "ds_read_b128 v[220:223], v240 offset:23552\n"
  "v_mul_f32 v176, v236, v237\n"
  "s_waitcnt lgkmcnt(3)\n"
  "v_mfma_f32_16x16x32_bf16 v[144:147], v[208:211], v[80:83], v[144:147]\n"
  "v_mul_f32 v180, v229, v231\n"
  "ds_read_b128 v[228:231], v244 offset:544\n"
  "v_fma_f32 v236, v225, v232, v233\n"
  "v_mfma_f32_16x16x32_bf16 v[148:151], v[208:211], v[84:87], v[148:151]\n"
  "ds_read_b128 v[208:211], v240 offset:24576\n"
  "v_fma_f32 v237, v225, v234, v235\n"
  "s_waitcnt lgkmcnt(4)\n"
  "v_mfma_f32_16x16x32_bf16 v[152:155], v[212:215], v[80:83], v[152:155]\n"
  "s_add_u32 m0, s45, 0x22000\n"
  "s_add_u32 s68, s44, 0x46000\n"
  "buffer_load_dwordx4 v240, s[36:39], s68 offen lds\n"
  "v_fmac_f32 v233, v227, v232\n"
  "v_mfma_f32_16x16x32_bf16 v[156:159], v[212:215], v[84:87], v[156:159]\n"
  "ds_read_b128 v[212:215], v240 offset:25600\n"
  "v_fmac_f32 v235, v227, v234\n"
  "v_fmac_f32 v236, v224, v177\n"
  "s_waitcnt lgkmcnt(4)\n"
  "v_mfma_f32_16x16x32_bf16 v[144:147], v[216:219], v[88:91], v[144:147]\n"
  "v_fmac_f32 v237, v224, v185\n"
  "v_fmac_f32 v233, v226, v181\n"
  "v_mfma_f32_16x16x32_bf16 v[148:151], v[216:219], v[92:95], v[148:151]\n"
  "ds_read_b128 v[216:219], v240 offset:26624\n"
  "v_fmac_f32 v235, v226, v189\n"
  "s_waitcnt lgkmcnt(4)\n"
  "v_mfma_f32_16x16x32_bf16 v[152:155], v[220:223], v[88:91], v[152:155]\n"
  "v_mul_f32 v177, v236, v237\n"
  "v_mul_f32 v181, v233, v235\n"
  "v_mfma_f32_16x16x32_bf16 v[156:159], v[220:223], v[92:95], v[156:159]\n"
  "ds_read_b128 v[220:223], v240 offset:27648\n"
  "ds_read_b128 v[232:235], v244 offset:560\n"
  "s_waitcnt lgkmcnt(5)\n"
  "v_fma_f32 v236, v225, v228, v229\n"
  "s_waitcnt lgkmcnt(4)\n"
  "v_mfma_f32_16x16x32_bf16 v[144:147], v[208:211], v[96:99], v[144:147]\n"
  "v_fma_f32 v237, v225, v230, v231\n"
  "v_fmac_f32 v229, v227, v228\n"
  "v_mfma_f32_16x16x32_bf16 v[148:151], v[208:211], v[100:103], v[148:151]\n"
  "ds_read_b128 v[208:211], v240 offset:28672\n"
  "v_fmac_f32 v231, v227, v230\n"
  "s_waitcnt lgkmcnt(4)\n"
  "v_mfma_f32_16x16x32_bf16 v[152:155], v[212:215], v[96:99], v[152:155]\n"
  "s_add_u32 m0, s45, 0\n"
  "s_add_u32 s68, s44, 0x48000\n"
  "buffer_load_dwordx4 v240, s[36:39], s68 offen lds\n"
  "v_fmac_f32 v236, v224, v178\n"
  "v_mfma_f32_16x16x32_bf16 v[156:159], v[212:215], v[100:103], v[156:159]\n"
  "ds_read_b128 v[212:215], v240 offset:29696\n"
  "v_fmac_f32 v237, v224, v186\n"
  "v_fmac_f32 v229, v226, v182\n"
  "s_waitcnt lgkmcnt(4)\n"
  "v_mfma_f32_16x16x32_bf16 v[144:147], v[216:219], v[104:107], v[144:147]\n"
  "v_fmac_f32 v231, v226, v190\n"
  "v_mul_f32 v178, v236, v237\n"
  "v_mfma_f32_16x16x32_bf16 v[148:151], v[216:219], v[108:111], v[148:151]\n"
  "ds_read_b128 v[216:219], v240 offset:30720\n"
  "v_mul_f32 v182, v229, v231\n"
  "s_waitcnt lgkmcnt(4)\n"
  "v_mfma_f32_16x16x32_bf16 v[152:155], v[220:223], v[104:107], v[152:155]\n"
  "s_waitcnt lgkmcnt(3)\n"
  "v_fma_f32 v236, v225, v232, v233\n"
  "v_fma_f32 v237, v225, v234, v235\n"
  "v_mfma_f32_16x16x32_bf16 v[156:159], v[220:223], v[108:111], v[156:159]\n"
  "ds_read_b128 v[220:223], v240 offset:31744\n"
  "v_fmac_f32 v233, v227, v232\n"
  "s_waitcnt lgkmcnt(3)\n"
  "v_mfma_f32_16x16x32_bf16 v[144:147], v[208:211], v[112:115], v[144:147]\n"
  "v_fmac_f32 v235, v227, v234\n"
  "v_fmac_f32 v236, v224, v179\n"
  "v_mfma_f32_16x16x32_bf16 v[148:151], v[208:211], v[116:119], v[148:151]\n"
  "ds_read_b128 v[208:211], v240 offset:32768\n"
  "v_fmac_f32 v237, v224, v187\n"
  "s_waitcnt lgkmcnt(3)\n"
  "v_mfma_f32_16x16x32_bf16 v[152:155], v[212:215], v[112:115], v[152:155]\n"
  "v_fmac_f32 v233, v226, v183\n"
  "v_fmac_f32 v235, v226, v191\n"
  "v_mfma_f32_16x16x32_bf16 v[156:159], v[212:215], v[116:119], v[156:159]\n"
  "ds_read_b128 v[212:215], v240 offset:33792\n"
  "v_mul_f32 v179, v236, v237\n"
  "s_waitcnt lgkmcnt(3)\n"
  "v_mfma_f32_16x16x32_bf16 v[144:147], v[216:219], v[120:123], v[144:147]\n"
  "v_mul_f32 v183, v233, v235\n"
  "v_cvt_pk_bf16_f32 v184, v176, v177\n"
  "v_mfma_f32_16x16x32_bf16 v[148:151], v[216:219], v[124:127], v[148:151]\n"
  "ds_read_b128 v[216:219], v240 offset:34816\n"
  "v_cvt_pk_bf16_f32 v185, v178, v179\n"
  "s_waitcnt lgkmcnt(3)\n"
  "v_mfma_f32_16x16x32_bf16 v[152:155], v[220:223], v[120:123], v[152:155]\n"
  "v_cvt_pk_bf16_f32 v186, v180, v181\n"
  "v_cvt_pk_bf16_f32 v187, v182, v183\n"
  "v_mfma_f32_16x16x32_bf16 v[156:159], v[220:223], v[124:127], v[156:159]\n"
  "ds_read_b128 v[220:223], v240 offset:35840\n"
  "ds_read_b128 v[228:231], v244 offset:768\n"
  "s_waitcnt lgkmcnt(4)\n"
  "v_mfma_f32_16x16x32_bf16 v[144:147], v[208:211], v[128:131], v[144:147]\n"
  "s_add_u32 s69, s48, 0x4000\n"
  "buffer_store_dwordx4 v[184:187], v240, s[36:39], s69 offen sc1\n"
  "v_mfma_f32_16x16x32_bf16 v[148:151], v[208:211], v[132:135], v[148:151]\n"
  "ds_read_b128 v[208:211], v240 offset:36864\n"
  "ds_read_b128 v[232:235], v244 offset:784\n"
  "s_waitcnt lgkmcnt(5)\n"
  "v_mfma_f32_16x16x32_bf16 v[152:155], v[212:215], v[128:131], v[152:155]\n"
  "v_mfma_f32_16x16x32_bf16 v[156:159], v[212:215], v[132:135], v[156:159]\n"
  "ds_read_b128 v[212:215], v240 offset:37888\n"
  "s_waitcnt lgkmcnt(5)\n"
  "v_mfma_f32_16x16x32_bf16 v[144:147], v[216:219], v[136:139], v[144:147]\n"
  "s_waitcnt lgkmcnt(3)\n"
  "v_fma_f32 v236, v225, v228, v229\n"
  "v_fma_f32 v237, v225, v230, v231\n"
  "v_mfma_f32_16x16x32_bf16 v[148:151], v[216:219], v[140:143], v[148:151]\n"
  "ds_read_b128 v[216:219], v240 offset:38912\n"
  "v_fmac_f32 v229, v227, v228\n"
  "v_mfma_f32_16x16x32_bf16 v[152:155], v[220:223], v[136:139], v[152:155]\n"
  "v_fmac_f32 v231, v227, v230\n"
  "v_fmac_f32 v236, v224, v192\n"
  "v_mfma_f32_16x16x32_bf16 v[156:159], v[220:223], v[140:143], v[156:159]\n"
  "ds_read_b128 v[220:223], v240 offset:39936\n"
  "v_fmac_f32 v237, v224, v200\n"
  "s_waitcnt lgkmcnt(4)\n"
  "v_mfma_f32_16x16x32_bf16 v[160:163], v[208:211], v[0:3], 0\n"
  "v_fmac_f32 v229, v226, v196\n"
  "v_fmac_f32 v231, v226, v204\n"
  "v_mfma_f32_16x16x32_bf16 v[164:167], v[208:211], v[4:7], 0\n"
  "ds_read_b128 v[208:211], v240 offset:40960\n"
  "v_mul_f32 v192, v236, v237\n"
  "s_waitcnt lgkmcnt(3)\n"
  "v_mfma_f32_16x16x32_bf16 v[168:171], v[212:215], v[0:3], 0\n"
  "v_mul_f32 v196, v229, v231\n"
  "ds_read_b128 v[228:231], v244 offset:800\n"
  "v_fma_f32 v236, v225, v232, v233\n"
  "v_mfma_f32_16x16x32_bf16 v[172:175], v[212:215], v[4:7], 0\n"
  "ds_read_b128 v[212:215], v240 offset:41984\n"
  "v_fma_f32 v237, v225, v234, v235\n"
  "s_waitcnt lgkmcnt(4)\n"
  "v_mfma_f32_16x16x32_bf16 v[160:163], v[216:219], v[8:11], v[160:163]\n"
  "v_fmac_f32 v233, v227, v232\n"
  "v_fmac_f32 v235, v227, v234\n"
  "v_mfma_f32_16x16x32_bf16 v[164:167], v[216:219], v[12:15], v[164:167]\n"
  "ds_read_b128 v[216:219], v240 offset:43008\n"
  "v_fmac_f32 v236, v224, v193\n"
  "s_waitcnt lgkmcnt(4)\n"
  "v_mfma_f32_16x16x32_bf16 v[168:171], v[220:223], v[8:11], v[168:171]\n"
  "v_fmac_f32 v237, v224, v201\n"
  "v_fmac_f32 v233, v226, v197\n"
  "v_mfma_f32_16x16x32_bf16 v[172:175], v[220:223], v[12:15], v[172:175]\n"
  "ds_read_b128 v[220:223], v240 offset:44032\n"
  "v_fmac_f32 v235, v226, v205\n"
  "s_waitcnt lgkmcnt(4)\n"
  "v_mfma_f32_16x16x32_bf16 v[160:163], v[208:211], v[16:19], v[160:163]\n"
  "v_mul_f32 v193, v236, v237\n"
  "v_mul_f32 v197, v233, v235\n"
  "v_mfma_f32_16x16x32_bf16 v[164:167], v[208:211], v[20:23], v[164:167]\n"
  "ds_read_b128 v[208:211], v240 offset:45056\n"
  "ds_read_b128 v[232:235], v244 offset:816\n"
  "s_waitcnt lgkmcnt(5)\n"
  "v_fma_f32 v236, v225, v228, v229\n"
  "s_waitcnt lgkmcnt(4)\n"
  "v_mfma_f32_16x16x32_bf16 v[168:171], v[212:215], v[16:19], v[168:171]\n"
  "v_fma_f32 v237, v225, v230, v231\n"
  "v_fmac_f32 v229, v227, v228\n"
  "v_mfma_f32_16x16x32_bf16 v[172:175], v[212:215], v[20:23], v[172:175]\n"
  "ds_read_b128 v[212:215], v240 offset:46080\n"
  "v_fmac_f32 v231, v227, v230\n"
  "s_waitcnt lgkmcnt(4)\n"
  "v_mfma_f32_16x16x32_bf16 v[160:163], v[216:219], v[24:27], v[160:163]\n"
  "v_fmac_f32 v236, v224, v194\n"
  "v_fmac_f32 v237, v224, v202\n"
  "v_mfma_f32_16x16x32_bf16 v[164:167], v[216:219], v[28:31], v[164:167]\n"
  "ds_read_b128 v[216:219], v240 offset:47104\n"
  "v_fmac_f32 v229, v226, v198\n"
  "s_waitcnt lgkmcnt(4)\n"
  "v_mfma_f32_16x16x32_bf16 v[168:171], v[220:223], v[24:27], v[168:171]\n"
  "v_fmac_f32 v231, v226, v206\n"
  "v_mul_f32 v194, v236, v237\n"
  "v_mfma_f32_16x16x32_bf16 v[172:175], v[220:223], v[28:31], v[172:175]\n"
  "ds_read_b128 v[220:223], v240 offset:48128\n"
  "v_mul_f32 v198, v229, v231\n"
  "s_waitcnt vmcnt(12)\n"
  "s_barrier\n"
  "s_waitcnt lgkmcnt(4)\n"
  "v_mfma_f32_16x16x32_bf16 v[160:163], v[208:211], v[32:35], v[160:163]\n"
  "s_waitcnt lgkmcnt(3)\n"
  "v_fma_f32 v236, v225, v232, v233\n"
  "v_fma_f32 v237, v225, v234, v235\n"
  "v_mfma_f32_16x16x32_bf16 v[164:167], v[208:211], v[36:39], v[164:167]\n"
  "ds_read_b128 v[208:211], v240 offset:49152\n"
  "v_fmac_f32 v233, v227, v232\n"
  "s_waitcnt lgkmcnt(3)\n"
  "v_mfma_f32_16x16x32_bf16 v[168:171], v[212:215], v[32:35], v[168:171]\n"
  "s_add_u32 m0, s45, 0x2000\n"
  "s_add_u32 s68, s44, 0x4a000\n"
  "buffer_load_dwordx4 v240, s[36:39], s68 offen lds\n"
  "v_fmac_f32 v235, v227, v234\n"
  "v_mfma_f32_16x16x32_bf16 v[172:175], v[212:215], v[36:39], v[172:175]\n"
  "ds_read_b128 v[212:215], v240 offset:50176\n"
  "v_fmac_f32 v236, v224, v195\n"
  "v_fmac_f32 v237, v224, v203\n"
  "s_waitcnt lgkmcnt(3)\n"
  "v_mfma_f32_16x16x32_bf16 v[160:163], v[216:219], v[40:43], v[160:163]\n"
  "v_fmac_f32 v233, v226, v199\n"
  "v_fmac_f32 v235, v226, v207\n"
  "v_mfma_f32_16x16x32_bf16 v[164:167], v[216:219], v[44:47], v[164:167]\n"
  "ds_read_b128 v[216:219], v240 offset:51200\n"
  "v_mul_f32 v195, v236, v237\n"
  "s_waitcnt lgkmcnt(3)\n"
  "v_mfma_f32_16x16x32_bf16 v[168:171], v[220:223], v[40:43], v[168:171]\n"
  "v_mul_f32 v199, v233, v235\n"
  "v_cvt_pk_bf16_f32 v200, v192, v193\n"
  "v_mfma_f32_16x16x32_bf16 v[172:175], v[220:223], v[44:47], v[172:175]\n"
  "ds_read_b128 v[220:223], v240 offset:52224\n"
  "v_cvt_pk_bf16_f32 v201, v194, v195\n"
  "s_waitcnt lgkmcnt(3)\n"
  "v_mfma_f32_16x16x32_bf16 v[160:163], v[208:211], v[48:51], v[160:163]\n"
  "v_cvt_pk_bf16_f32 v202, v196, v197\n"
  "v_cvt_pk_bf16_f32 v203, v198, v199\n"
  "v_mfma_f32_16x16x32_bf16 v[164:167], v[208:211], v[52:55], v[164:167]\n"
  "ds_read_b128 v[208:211], v240 offset:53248\n"
  "s_waitcnt lgkmcnt(3)\n"
  "v_mfma_f32_16x16x32_bf16 v[168:171], v[212:215], v[48:51], v[168:171]\n"
  "s_add_u32 m0, s45, 0x4000\n"
  "s_add_u32 s68, s44, 0x4c000\n"
  "buffer_load_dwordx4 v240, s[36:39], s68 offen lds\n"
  "v_mfma_f32_16x16x32_bf16 v[172:175], v[212:215], v[52:55], v[172:175]\n"
  "ds_read_b128 v[212:215], v240 offset:54272\n"
  "ds_read_b128 v[228:231], v244 offset:1024\n"
  "s_waitcnt lgkmcnt(4)\n"
  "v_mfma_f32_16x16x32_bf16 v[160:163], v[216:219], v[56:59], v[160:163]\n"
  "s_add_u32 s69, s48, 0x6000\n"
  "buffer_store_dwordx4 v[200:203], v240, s[36:39], s69 offen sc1\n"
  "v_mfma_f32_16x16x32_bf16 v[164:167], v[216:219], v[60:63], v[164:167]\n"
  "ds_read_b128 v[216:219], v240 offset:55296\n"
  "ds_read_b128 v[232:235], v244 offset:1040\n"
  "s_waitcnt lgkmcnt(5)\n"
  "v_mfma_f32_16x16x32_bf16 v[168:171], v[220:223], v[56:59], v[168:171]\n"
  "v_mfma_f32_16x16x32_bf16 v[172:175], v[220:223], v[60:63], v[172:175]\n"
  "ds_read_b128 v[220:223], v240 offset:56320\n"
  "s_waitcnt lgkmcnt(5)\n"
  "v_mfma_f32_16x16x32_bf16 v[160:163], v[208:211], v[64:67], v[160:163]\n"
  "s_waitcnt lgkmcnt(3)\n"
  "v_fma_f32 v236, v225, v228, v229\n"
  "v_fma_f32 v237, v225, v230, v231\n"
  "v_mfma_f32_16x16x32_bf16 v[164:167], v[208:211], v[68:71], v[164:167]\n"
  "ds_read_b128 v[208:211], v240 offset:57344\n"
  "v_fmac_f32 v229, v227, v228\n"
  "v_mfma_f32_16x16x32_bf16 v[168:171], v[212:215], v[64:67], v[168:171]\n"
  "s_add_u32 m0, s45, 0x6000\n"
  "s_add_u32 s68, s44, 0x4e000\n"
  "buffer_load_dwordx4 v240, s[36:39], s68 offen lds\n"
  "v_fmac_f32 v231, v227, v230\n"
  "v_mfma_f32_16x16x32_bf16 v[172:175], v[212:215], v[68:71], v[172:175]\n"
  "ds_read_b128 v[212:215], v240 offset:58368\n"
  "v_fmac_f32 v236, v224, v144\n"
  "v_fmac_f32 v237, v224, v152\n"
  "s_waitcnt lgkmcnt(4)\n"
  "v_mfma_f32_16x16x32_bf16 v[160:163], v[216:219], v[72:75], v[160:163]\n"
  "v_fmac_f32 v229, v226, v148\n"
  "v_fmac_f32 v231, v226, v156\n"
  "v_mfma_f32_16x16x32_bf16 v[164:167], v[216:219], v[76:79], v[164:167]\n"
  "ds_read_b128 v[216:219], v240 offset:59392\n"
  "v_mul_f32 v144, v236, v237\n"
  "s_waitcnt lgkmcnt(3)\n"
  "v_mfma_f32_16x16x32_bf16 v[168:171], v[220:223], v[72:75], v[168:171]\n"
  "v_mul_f32 v148, v229, v231\n"
  "ds_read_b128 v[228:231], v244 offset:1056\n"
  "v_fma_f32 v236, v225, v232, v233\n"
  "v_mfma_f32_16x16x32_bf16 v[172:175], v[220:223], v[76:79], v[172:175]\n"
  "ds_read_b128 v[220:223], v240 offset:60416\n"
  "v_fma_f32 v237, v225, v234, v235\n"
  "s_waitcnt lgkmcnt(4)\n"
  "v_mfma_f32_16x16x32_bf16 v[160:163], v[208:211], v[80:83], v[160:163]\n"
  "v_fmac_f32 v233, v227, v232\n"
  "v_fmac_f32 v235, v227, v234\n"
  "v_mfma_f32_16x16x32_bf16 v[164:167], v[208:211], v[84:87], v[164:167]\n"
  "ds_read_b128 v[208:211], v240 offset:61440\n"
  "v_fmac_f32 v236, v224, v145\n"
  "s_waitcnt lgkmcnt(4)\n"
  "v_mfma_f32_16x16x32_bf16 v[168:171], v[212:215], v[80:83], v[168:171]\n"
  "s_add_u32 m0, s45, 0x8000\n"
  "s_nop 0\n"
  "buffer_load_dwordx4 v240, s[36:39], s53 offen lds\n"
  "v_fmac_f32 v237, v224, v153\n"
  "v_mfma_f32_16x16x32_bf16 v[172:175], v[212:215], v[84:87], v[172:175]\n"
  "ds_read_b128 v[212:215], v240 offset:62464\n"
  "v_fmac_f32 v233, v226, v149\n"
  "v_fmac_f32 v235, v226, v157\n"
  "s_waitcnt lgkmcnt(4)\n"
  "v_mfma_f32_16x16x32_bf16 v[160:163], v[216:219], v[88:91], v[160:163]\n"
  "v_mul_f32 v145, v236, v237\n"
  "v_mul_f32 v149, v233, v235\n"
  "v_mfma_f32_16x16x32_bf16 v[164:167], v[216:219], v[92:95], v[164:167]\n"
  "ds_read_b128 v[216:219], v240 offset:63488\n"
  "ds_read_b128 v[232:235], v244 offset:1072\n"
  "s_waitcnt lgkmcnt(5)\n"
  "v_fma_f32 v236, v225, v228, v229\n"
  "s_waitcnt lgkmcnt(4)\n"
  "v_mfma_f32_16x16x32_bf16 v[168:171], v[220:223], v[88:91], v[168:171]\n"
  "v_fma_f32 v237, v225, v230, v231\n"
  "v_fmac_f32 v229, v227, v228\n"
  "v_mfma_f32_16x16x32_bf16 v[172:175], v[220:223], v[92:95], v[172:175]\n"
  "ds_read_b128 v[220:223], v240 offset:64512\n"
  "v_fmac_f32 v231, v227, v230\n"
  "s_waitcnt lgkmcnt(4)\n"
  "v_mfma_f32_16x16x32_bf16 v[160:163], v[208:211], v[96:99], v[160:163]\n"
  "v_fmac_f32 v236, v224, v146\n"
  "v_fmac_f32 v237, v224, v154\n"
  "v_mfma_f32_16x16x32_bf16 v[164:167], v[208:211], v[100:103], v[164:167]\n"
  "ds_read_b128 v[208:211], v241 offset:0\n"
  "v_fmac_f32 v229, v226, v150\n"
  "s_waitcnt lgkmcnt(4)\n"
  "v_mfma_f32_16x16x32_bf16 v[168:171], v[212:215], v[96:99], v[168:171]\n"
  "v_fmac_f32 v231, v226, v158\n"
  "v_mul_f32 v146, v236, v237\n"
  "v_mfma_f32_16x16x32_bf16 v[172:175], v[212:215], v[100:103], v[172:175]\n"
  "ds_read_b128 v[212:215], v241 offset:1024\n"
  "v_mul_f32 v150, v229, v231\n"
  "s_waitcnt lgkmcnt(4)\n"
  "v_mfma_f32_16x16x32_bf16 v[160:163], v[216:219], v[104:107], v[160:163]\n"
  "s_waitcnt lgkmcnt(3)\n"
  "v_fma_f32 v236, v225, v232, v233\n"
  "v_fma_f32 v237, v225, v234, v235\n"
  "v_mfma_f32_16x16x32_bf16 v[164:167], v[216:219], v[108:111], v[164:167]\n"
  "ds_read_b128 v[216:219], v241 offset:2048\n"
  "v_fmac_f32 v233, v227, v232\n"
  "s_waitcnt lgkmcnt(3)\n"
  "v_mfma_f32_16x16x32_bf16 v[168:171], v[220:223], v[104:107], v[168:171]\n"
  "v_fmac_f32 v235, v227, v234\n"
  "v_fmac_f32 v236, v224, v147\n"
  "v_mfma_f32_16x16x32_bf16 v[172:175], v[220:223], v[108:111], v[172:175]\n"
  "ds_read_b128 v[220:223], v241 offset:3072\n"
  "v_fmac_f32 v237, v224, v155\n"
  "s_waitcnt lgkmcnt(3)\n"
  "v_mfma_f32_16x16x32_bf16 v[160:163], v[208:211], v[112:115], v[160:163]\n"
  "v_fmac_f32 v233, v226, v151\n"
  "v_fmac_f32 v235, v226, v159\n"
  "v_mfma_f32_16x16x32_bf16 v[164:167], v[208:211], v[116:119], v[164:167]\n"
  "ds_read_b128 v[208:211], v241 offset:4096\n"
  "v_mul_f32 v147, v236, v237\n"
  "s_waitcnt lgkmcnt(3)\n"
  "v_mfma_f32_16x16x32_bf16 v[168:171], v[212:215], v[112:115], v[168:171]\n"
  "v_mul_f32 v151, v233, v235\n"
  "v_cvt_pk_bf16_f32 v152, v144, v145\n"
  "v_mfma_f32_16x16x32_bf16 v[172:175], v[212:215], v[116:119], v[172:175]\n"
  "ds_read_b128 v[212:215], v241 offset:5120\n"
  "v_cvt_pk_bf16_f32 v153, v146, v147\n"
  "s_waitcnt lgkmcnt(3)\n"
  "v_mfma_f32_16x16x32_bf16 v[160:163], v[216:219], v[120:123], v[160:163]\n"
  "v_cvt_pk_bf16_f32 v154, v148, v149\n"
  "v_cvt_pk_bf16_f32 v155, v150, v151\n"
  "v_mfma_f32_16x16x32_bf16 v[164:167], v[216:219], v[124:127], v[164:167]\n"
  "ds_read_b128 v[216:219], v241 offset:6144\n"
  "s_waitcnt lgkmcnt(3)\n"
  "v_mfma_f32_16x16x32_bf16 v[168:171], v[220:223], v[120:123], v[168:171]\n"
  "s_add_u32 s69, s48, 0x8000\n"
  "buffer_store_dwordx4 v[152:155], v240, s[36:39], s69 offen sc1\n"
  "v_mfma_f32_16x16x32_bf16 v[172:175], v[220:223], v[124:127], v[172:175]\n"
  "ds_read_b128 v[220:223], v241 offset:7168\n"
  "s_waitcnt lgkmcnt(3)\n"
  "v_mfma_f32_16x16x32_bf16 v[160:163], v[208:211], v[128:131], v[160:163]\n"
  "v_mfma_f32_16x16x32_bf16 v[164:167], v[208:211], v[132:135], v[164:167]\n"
  "ds_read_b128 v[208:211], v241 offset:8192\n"
  "s_waitcnt lgkmcnt(3)\n"
  "v_mfma_f32_16x16x32_bf16 v[168:171], v[212:215], v[128:131], v[168:171]\n"
  "v_mfma_f32_16x16x32_bf16 v[172:175], v[212:215], v[132:135], v[172:175]\n"
  "ds_read_b128 v[212:215], v241 offset:9216\n"
  "s_waitcnt lgkmcnt(3)\n"
  "v_mfma_f32_16x16x32_bf16 v[160:163], v[216:219], v[136:139], v[160:163]\n"
  "ds_read_b128 v[228:231], v244 offset:1280\n"
  "v_mfma_f32_16x16x32_bf16 v[164:167], v[216:219], v[140:143], v[164:167]\n"
  "ds_read_b128 v[216:219], v241 offset:10240\n"
  "s_waitcnt lgkmcnt(4)\n"
  "v_mfma_f32_16x16x32_bf16 v[168:171], v[220:223], v[136:139], v[168:171]\n"
  "ds_read_b128 v[232:235], v244 offset:1296\n"
  "v_mfma_f32_16x16x32_bf16 v[172:175], v[220:223], v[140:143], v[172:175]\n"
  "ds_read_b128 v[220:223], v241 offset:11264\n"
  "s_waitcnt lgkmcnt(5)\n"
  "v_mfma_f32_16x16x32_bf16 v[144:147], v[208:211], v[0:3], 0\n"
  "v_mfma_f32_16x16x32_bf16 v[148:151], v[208:211], v[4:7], 0\n"
  "ds_read_b128 v[208:211], v241 offset:12288\n"
  "s_waitcnt lgkmcnt(5)\n"
  "v_mfma_f32_16x16x32_bf16 v[152:155], v[212:215], v[0:3], 0\n"
  "s_waitcnt lgkmcnt(4)\n"
  "v_fma_f32 v236, v225, v228, v229\n"
  "v_fma_f32 v237, v225, v230, v231\n"
  "v_mfma_f32_16x16x32_bf16 v[156:159], v[212:215], v[4:7], 0\n"
  "ds_read_b128 v[212:215], v241 offset:13312\n"
  "v_fmac_f32 v229, v227, v228\n"
  "s_waitcnt lgkmcnt(4)\n"
  "v_mfma_f32_16x16x32_bf16 v[144:147], v[216:219], v[8:11], v[144:147]\n"
  "v_fmac_f32 v231, v227, v230\n"
  "v_fmac_f32 v236, v224, v160\n"
  "v_mfma_f32_16x16x32_bf16 v[148:151], v[216:219], v[12:15], v[148:151]\n"
  "ds_read_b128 v[216:219], v241 offset:14336\n"
  "v_fmac_f32 v237, v224, v168\n"
  "s_waitcnt lgkmcnt(3)\n"
  "v_mfma_f32_16x16x32_bf16 v[152:155], v[220:223], v[8:11], v[152:155]\n"
  "v_fmac_f32 v229, v226, v164\n"
  "v_fmac_f32 v231, v226, v172\n"
  "v_mfma_f32_16x16x32_bf16 v[156:159], v[220:223], v[12:15], v[156:159]\n"
  "ds_read_b128 v[220:223], v241 offset:15360\n"
  "v_mul_f32 v160, v236, v237\n"
  "s_waitcnt vmcnt(0)\n"
  "s_barrier\n"
  "s_cmp_eq_u32 s45, 0\n"
  "s_cbranch_scc1 .Lk34_pub220\n"
  ".Lk34_pub220_r:\n"
  "s_waitcnt lgkmcnt(3)\n"
  "v_mfma_f32_16x16x32_bf16 v[144:147], v[208:211], v[16:19], v[144:147]\n"
  "v_mul_f32 v164, v229, v231\n"
  "ds_read_b128 v[228:231], v244 offset:1312\n"
  "v_fma_f32 v236, v225, v232, v233\n"
  "v_mfma_f32_16x16x32_bf16 v[148:151], v[208:211], v[20:23], v[148:151]\n"
  "ds_read_b128 v[208:211], v241 offset:16384\n"
  "v_fma_f32 v237, v225, v234, v235\n"
  "s_waitcnt lgkmcnt(4)\n"
  "v_mfma_f32_16x16x32_bf16 v[152:155], v[212:215], v[16:19], v[152:155]\n"
  "s_add_u32 m0, s45, 0xa000\n"
  "s_add_u32 s68, s52, 0x52000\n"
  "buffer_load_dwordx4 v240, s[36:39], s68 offen lds\n"
  "v_fmac_f32 v233, v227, v232\n"
  "v_mfma_f32_16x16x32_bf16 v[156:159], v[212:215], v[20:23], v[156:159]\n"
  "ds_read_b128 v[212:215], v241 offset:17408\n"
  "v_fmac_f32 v235, v227, v234\n"
  "v_fmac_f32 v236, v224, v161\n"
  "s_waitcnt lgkmcnt(4)\n"
  "v_mfma_f32_16x16x32_bf16 v[144:147], v[216:219], v[24:27], v[144:147]\n"
  "v_fmac_f32 v237, v224, v169\n"
  "v_fmac_f32 v233, v226, v165\n"
  "v_mfma_f32_16x16x32_bf16 v[148:151], v[216:219], v[28:31], v[148:151]\n"
  "ds_read_b128 v[216:219], v241 offset:18432\n"
  "v_fmac_f32 v235, v226, v173\n"
  "s_waitcnt lgkmcnt(4)\n"
  "v_mfma_f32_16x16x32_bf16 v[152:155], v[220:223], v[24:27], v[152:155]\n"
  "v_mul_f32 v161, v236, v237\n"
  "v_mul_f32 v165, v233, v235\n"
  "v_mfma_f32_16x16x32_bf16 v[156:159], v[220:223], v[28:31], v[156:159]\n"
  "ds_read_b128 v[220:223], v241 offset:19456\n"
  "ds_read_b128 v[232:235], v244 offset:1328\n"
  "s_waitcnt lgkmcnt(5)\n"
  "v_fma_f32 v236, v225, v228, v229\n"
  "s_waitcnt lgkmcnt(4)\n"
  "v_mfma_f32_16x16x32_bf16 v[144:147], v[208:211], v[32:35], v[144:147]\n"
  "v_fma_f32 v237, v225, v230, v231\n"
  "v_fmac_f32 v229, v227, v228\n"
  "v_mfma_f32_16x16x32_bf16 v[148:151], v[208:211], v[36:39], v[148:151]\n"
  "ds_read_b128 v[208:211], v241 offset:20480\n"
  "v_fmac_f32 v231, v227, v230\n"
  "s_waitcnt lgkmcnt(4)\n"
  "v_mfma_f32_16x16x32_bf16 v[152:155], v[212:215], v[32:35], v[152:155]\n"
  "s_add_u32 m0, s45, 0xc000\n"
  "s_add_u32 s68, s52, 0x54000\n"
  "buffer_load_dwordx4 v240, s[36:39], s68 offen lds\n"
  "v_fmac_f32 v236, v224, v162\n"
  "v_mfma_f32_16x16x32_bf16 v[156:159], v[212:215], v[36:39], v[156:159]\n"
  "ds_read_b128 v[212:215], v241 offset:21504\n"
  "v_fmac_f32 v237, v224, v170\n"
  "v_fmac_f32 v229, v226, v166\n"
  "s_waitcnt lgkmcnt(4)\n"
  "v_mfma_f32_16x16x32_bf16 v[144:147], v[216:219], v[40:43], v[144:147]\n"
  "v_fmac_f32 v231, v226, v174\n"
  "v_mul_f32 v162, v236, v237\n"
  "v_mfma_f32_16x16x32_bf16 v[148:151], v[216:219], v[44:47], v[148:151]\n"
  "ds_read_b128 v[216:219], v241 offset:22528\n"
  "v_mul_f32 v166, v229, v231\n"
  "s_waitcnt lgkmcnt(4)\n"
  "v_mfma_f32_16x16x32_bf16 v[152:155], v[220:223], v[40:43], v[152:155]\n"
  "s_waitcnt lgkmcnt(3)\n"
  "v_fma_f32 v236, v225, v232, v233\n"
  "v_fma_f32 v237, v225, v234, v235\n"
  "v_mfma_f32_16x16x32_bf16 v[156:159], v[220:223], v[44:47], v[156:159]\n"
  "ds_read_b128 v[220:223], v241 offset:23552\n"
  "v_fmac_f32 v233, v227, v232\n"
  "s_waitcnt lgkmcnt(3)\n"
  "v_mfma_f32_16x16x32_bf16 v[144:147], v[208:211], v[48:51], v[144:147]\n"
  "v_fmac_f32 v235, v227, v234\n"
  "v_fmac_f32 v236, v224, v163\n"
  "v_mfma_f32_16x16x32_bf16 v[148:151], v[208:211], v[52:55], v[148:151]\n"
  "ds_read_b128 v[208:211], v241 offset:24576\n"
  "v_fmac_f32 v237, v224, v171\n"
  "s_waitcnt lgkmcnt(3)\n"
  "v_mfma_f32_16x16x32_bf16 v[152:155], v[212:215], v[48:51], v[152:155]\n"
  "s_add_u32 m0, s45, 0xe000\n"
  "s_add_u32 s68, s52, 0x56000\n"
  "buffer_load_dwordx4 v240, s[36:39], s68 offen lds\n"
  "v_fmac_f32 v233, v226, v167\n"
  "v_mfma_f32_16x16x32_bf16 v[156:159], v[212:215], v[52:55], v[156:159]\n"
  "ds_read_b128 v[212:215], v241 offset:25600\n"
  "v_fmac_f32 v235, v226, v175\n"
  "v_mul_f32 v163, v236, v237\n"
  "s_waitcnt lgkmcnt(3)\n"
  "v_mfma_f32_16x16x32_bf16 v[144:147], v[216:219], v[56:59], v[144:147]\n"
  "v_mul_f32 v167, v233, v235\n"
  "v_cvt_pk_bf16_f32 v168, v160, v161\n"
  "v_mfma_f32_16x16x32_bf16 v[148:151], v[216:219], v[60:63], v[148:151]\n"
  "ds_read_b128 v[216:219], v241 offset:26624\n"
  "v_cvt_pk_bf16_f32 v169, v162, v163\n"
  "s_waitcnt lgkmcnt(3)\n"
  "v_mfma_f32_16x16x32_bf16 v[152:155], v[220:223], v[56:59], v[152:155]\n"
  "v_cvt_pk_bf16_f32 v170, v164, v165\n"
  "v_cvt_pk_bf16_f32 v171, v166, v167\n"
  "v_mfma_f32_16x16x32_bf16 v[156:159], v[220:223], v[60:63], v[156:159]\n"
  "ds_read_b128 v[220:223], v241 offset:27648\n"
  "s_waitcnt lgkmcnt(3)\n"
  "v_mfma_f32_16x16x32_bf16 v[144:147], v[208:211], v[64:67], v[144:147]\n"
  "s_add_u32 s69, s48, 0xa000\n"
  "buffer_store_dwordx4 v[168:171], v240, s[36:39], s69 offen sc1\n"
  "v_mfma_f32_16x16x32_bf16 v[148:151], v[208:211], v[68:71], v[148:151]\n"
  "ds_read_b128 v[208:211], v241 offset:28672\n"
  "s_waitcnt lgkmcnt(3)\n"
  "v_mfma_f32_16x16x32_bf16 v[152:155], v[212:215], v[64:67], v[152:155]\n"
  "s_add_u32 m0, s45, 0x10000\n"
  "s_add_u32 s68, s52, 0x58000\n"
  "buffer_load_dwordx4 v240, s[36:39], s68 offen lds\n"
  "v_mfma_f32_16x16x32_bf16 v[156:159], v[212:215], v[68:71], v[156:159]\n"
  "ds_read_b128 v[212:215], v241 offset:29696\n"
  "s_waitcnt lgkmcnt(3)\n"
  "v_mfma_f32_16x16x32_bf16 v[144:147], v[216:219], v[72:75], v[144:147]\n"
  "v_mfma_f32_16x16x32_bf16 v[148:151], v[216:219], v[76:79], v[148:151]\n"
  "ds_read_b128 v[216:219], v241 offset:30720\n"
  "s_waitcnt lgkmcnt(3)\n"
  "v_mfma_f32_16x16x32_bf16 v[152:155], v[220:223], v[72:75], v[152:155]\n"
  "v_mfma_f32_16x16x32_bf16 v[156:159], v[220:223], v[76:79], v[156:159]\n"
  "ds_read_b128 v[220:223], v241 offset:31744\n"
  "s_waitcnt lgkmcnt(3)\n"
  "v_mfma_f32_16x16x32_bf16 v[144:147], v[208:211], v[80:83], v[144:147]\n"
  "v_mfma_f32_16x16x32_bf16 v[148:151], v[208:211], v[84:87], v[148:151]\n"
  "ds_read_b128 v[208:211], v241 offset:32768\n"
  "s_waitcnt lgkmcnt(3)\n"
  "v_mfma_f32_16x16x32_bf16 v[152:155], v[212:215], v[80:83], v[152:155]\n"
  "v_mfma_f32_16x16x32_bf16 v[156:159], v[212:215], v[84:87], v[156:159]\n"
  "ds_read_b128 v[212:215], v241 offset:33792\n"
  "s_waitcnt lgkmcnt(3)\n"
  "v_mfma_f32_16x16x32_bf16 v[144:147], v[216:219], v[88:91], v[144:147]\n"
  "buffer_load_dword v249, v250, s[36:39], s55 offen sc1\n"
  "v_mfma_f32_16x16x32_bf16 v[148:151], v[216:219], v[92:95], v[148:151]\n"
  "ds_read_b128 v[216:219], v241 offset:34816\n"
  "s_waitcnt lgkmcnt(3)\n"
  "v_mfma_f32_16x16x32_bf16 v[152:155], v[220:223], v[88:91], v[152:155]\n"
  "v_mfma_f32_16x16x32_bf16 v[156:159], v[220:223], v[92:95], v[156:159]\n"
  "ds_read_b128 v[220:223], v241 offset:35840\n"
  "s_waitcnt lgkmcnt(3)\n"
  "v_mfma_f32_16x16x32_bf16 v[144:147], v[208:211], v[96:99], v[144:147]\n"
  "v_mfma_f32_16x16x32_bf16 v[148:151], v[208:211], v[100:103], v[148:151]\n"
  "ds_read_b128 v[208:211], v241 offset:36864\n"
  "s_waitcnt lgkmcnt(3)\n"
  "v_mfma_f32_16x16x32_bf16 v[152:155], v[212:215], v[96:99], v[152:155]\n"
  "v_mfma_f32_16x16x32_bf16 v[156:159], v[212:215], v[100:103], v[156:159]\n"
  "ds_read_b128 v[212:215], v241 offset:37888\n"
  "s_waitcnt lgkmcnt(3)\n"
  "v_mfma_f32_16x16x32_bf16 v[144:147], v[216:219], v[104:107], v[144:147]\n"
  "v_mfma_f32_16x16x32_bf16 v[148:151], v[216:219], v[108:111], v[148:151]\n"
  "ds_read_b128 v[216:219], v241 offset:38912\n"
  "s_waitcnt lgkmcnt(3)\n"
  "v_mfma_f32_16x16x32_bf16 v[152:155], v[220:223], v[104:107], v[152:155]\n"
  "v_mfma_f32_16x16x32_bf16 v[156:159], v[220:223], v[108:111], v[156:159]\n"
  "ds_read_b128 v[220:223], v241 offset:39936\n"
  "s_waitcnt lgkmcnt(3)\n"
  "v_mfma_f32_16x16x32_bf16 v[144:147], v[208:211], v[112:115], v[144:147]\n"
  "v_mfma_f32_16x16x32_bf16 v[148:151], v[208:211], v[116:119], v[148:151]\n"
  "ds_read_b128 v[208:211], v241 offset:40960\n"
  "s_waitcnt lgkmcnt(3)\n"
  "v_mfma_f32_16x16x32_bf16 v[152:155], v[212:215], v[112:115], v[152:155]\n"
  "v_mfma_f32_16x16x32_bf16 v[156:159], v[212:215], v[116:119], v[156:159]\n"
  "ds_read_b128 v[212:215], v241 offset:41984\n"
  "s_waitcnt lgkmcnt(3)\n"
  "v_mfma_f32_16x16x32_bf16 v[144:147], v[216:219], v[120:123], v[144:147]\n"
  "v_mfma_f32_16x16x32_bf16 v[148:151], v[216:219], v[124:127], v[148:151]\n"
  "ds_read_b128 v[216:219], v241 offset:43008\n"
  "s_waitcnt lgkmcnt(3)\n"
  "v_mfma_f32_16x16x32_bf16 v[152:155], v[220:223], v[120:123], v[152:155]\n"
  "v_mfma_f32_16x16x32_bf16 v[156:159], v[220:223], v[124:127], v[156:159]\n"
  "ds_read_b128 v[220:223], v241 offset:44032\n"
  "s_waitcnt lgkmcnt(3)\n"
  "v_mfma_f32_16x16x32_bf16 v[144:147], v[208:211], v[128:131], v[144:147]\n"
  "v_mfma_f32_16x16x32_bf16 v[148:151], v[208:211], v[132:135], v[148:151]\n"
  "ds_read_b128 v[208:211], v241 offset:45056\n"
  "s_waitcnt lgkmcnt(3)\n"
  "v_mfma_f32_16x16x32_bf16 v[152:155], v[212:215], v[128:131], v[152:155]\n"
  "v_mfma_f32_16x16x32_bf16 v[156:159], v[212:215], v[132:135], v[156:159]\n"
  "ds_read_b128 v[212:215], v241 offset:46080\n"
  "s_waitcnt lgkmcnt(3)\n"
  "v_mfma_f32_16x16x32_bf16 v[144:147], v[216:219], v[136:139], v[144:147]\n"
  "ds_read_b128 v[228:231], v244 offset:1536\n"
  "v_mfma_f32_16x16x32_bf16 v[148:151], v[216:219], v[140:143], v[148:151]\n"
  "ds_read_b128 v[216:219], v241 offset:47104\n"
  "s_waitcnt lgkmcnt(4)\n"
  "v_mfma_f32_16x16x32_bf16 v[152:155], v[220:223], v[136:139], v[152:155]\n"
  "ds_read_b128 v[232:235], v244 offset:1552\n"
  "v_mfma_f32_16x16x32_bf16 v[156:159], v[220:223], v[140:143], v[156:159]\n"
  "ds_read_b128 v[220:223], v241 offset:48128\n"
  "s_barrier\n"
  "s_waitcnt lgkmcnt(5)\n"
  "v_mfma_f32_16x16x32_bf16 v[160:163], v[208:211], v[0:3], 0\n"
  "s_waitcnt vmcnt(0)\n"
  "v_cmp_gt_u32 vcc, 5, v249\n"
  "s_cbranch_vccnz .Lk34_fl5\n"
  ".Lk34_fl5_r:\n"
  "v_mfma_f32_16x16x32_bf16 v[164:167], v[208:211], v[4:7], 0\n"
  "ds_read_b128 v[208:211], v241 offset:49152\n"
  "s_waitcnt lgkmcnt(5)\n"
  "v_mfma_f32_16x16x32_bf16 v[168:171], v[212:215], v[0:3], 0\n"
  "s_add_u32 m0, s45, 0x12000\n"
  "s_add_u32 s68, s52, 0x5a000\n"
  "buffer_load_dwordx4 v240, s[36:39], s68 offen lds\n"
  "s_waitcnt lgkmcnt(4)\n"
  "v_fma_f32 v236, v225, v228, v229\n"
  "v_mfma_f32_16x16x32_bf16 v[172:175], v[212:215], v[4:7], 0\n"
  "ds_read_b128 v[212:215], v241 offset:50176\n"
  "v_fma_f32 v237, v225, v230, v231\n"
  "v_fmac_f32 v229, v227, v228\n"
  "s_waitcnt lgkmcnt(4)\n"
  "v_mfma_f32_16x16x32_bf16 v[160:163], v[216:219], v[8:11], v[160:163]\n"
  "v_fmac_f32 v231, v227, v230\n"
  "v_fmac_f32 v236, v224, v144\n"
  "v_fmac_f32 v237, v224, v152\n"
  "v_mfma_f32_16x16x32_bf16 v[164:167], v[216:219], v[12:15], v[164:167]\n"
  "ds_read_b128 v[216:219], v241 offset:51200\n"
  "s_add_u32 s71, s56, 0\n"
  "buffer_load_dwordx4 v[176:179], v240, s[36:39], s71 offen sc1\n"
  "s_waitcnt lgkmcnt(3)\n"
  "v_mfma_f32_16x16x32_bf16 v[168:171], v[220:223], v[8:11], v[168:171]\n"
  "v_fmac_f32 v229, v226, v148\n"
  "v_fmac_f32 v231, v226, v156\n"
  "v_mul_f32 v144, v236, v237\n"
  "v_mfma_f32_16x16x32_bf16 v[172:175], v[220:223], v[12:15], v[172:175]\n"
  "ds_read_b128 v[220:223], v241 offset:52224\n"
  "buffer_load_dwordx4 v[180:183], v240, s[36:39], s71 offen offset:1024 sc1\n"
  "s_waitcnt lgkmcnt(3)\n"
  "v_mfma_f32_16x16x32_bf16 v[160:163], v[208:211], v[16:19], v[160:163]\n"
  "v_mul_f32 v148, v229, v231\n"
  "ds_read_b128 v[228:231], v244 offset:1568\n"
  "v_fma_f32 v236, v225, v232, v233\n"
  "v_fma_f32 v237, v225, v234, v235\n"
  "v_mfma_f32_16x16x32_bf16 v[164:167], v[208:211], v[20:23], v[164:167]\n"
  "ds_read_b128 v[208:211], v241 offset:53248\n"
  "buffer_load_dwordx4 v[184:187], v240, s[36:39], s71 offen offset:2048 sc1\n"
  "s_waitcnt lgkmcnt(4)\n"
  "v_mfma_f32_16x16x32_bf16 v[168:171], v[212:215], v[16:19], v[168:171]\n"
  "s_add_u32 m0, s45, 0x14000\n"
  "s_add_u32 s68, s52, 0x5c000\n"
  "buffer_load_dwordx4 v240, s[36:39], s68 offen lds\n"
  "v_fmac_f32 v233, v227, v232\n"
  "v_mfma_f32_16x16x32_bf16 v[172:175], v[212:215], v[20:23], v[172:175]\n"
  "ds_read_b128 v[212:215], v241 offset:54272\n"
  "v_fmac_f32 v235, v227, v234\n"
  "v_fmac_f32 v236, v224, v145\n"
  "s_waitcnt lgkmcnt(4)\n"
  "v_mfma_f32_16x16x32_bf16 v[160:163], v[216:219], v[24:27], v[160:163]\n"
  "v_fmac_f32 v237, v224, v153\n"
  "v_fmac_f32 v233, v226, v149\n"
  "v_fmac_f32 v235, v226, v157\n"
  "v_mfma_f32_16x16x32_bf16 v[164:167], v[216:219], v[28:31], v[164:167]\n"
  "ds_read_b128 v[216:219], v241 offset:55296\n"
  "buffer_load_dwordx4 v[188:191], v240, s[36:39], s71 offen offset:3072 sc1\n"
  "s_waitcnt lgkmcnt(4)\n"
  "v_mfma_f32_16x16x32_bf16 v[168:171], v[220:223], v[24:27], v[168:171]\n"
  "v_mul_f32 v145, v236, v237\n"
  "v_mul_f32 v149, v233, v235\n"
  "ds_read_b128 v[232:235], v244 offset:1584\n"
  "s_waitcnt lgkmcnt(4)\n"
  "v_fma_f32 v236, v225, v228, v229\n"
  "v_mfma_f32_16x16x32_bf16 v[172:175], v[220:223], v[28:31], v[172:175]\n"
  "ds_read_b128 v[220:223], v241 offset:56320\n"
  "s_add_u32 s71, s56, 0x2000\n"
  "buffer_load_dwordx4 v[192:195], v240, s[36:39], s71 offen sc1\n"
  "s_waitcnt lgkmcnt(4)\n"
  "v_mfma_f32_16x16x32_bf16 v[160:163], v[208:211], v[32:35], v[160:163]\n"
  "v_fma_f32 v237, v225, v230, v231\n"
  "v_fmac_f32 v229, v227, v228\n"
  "v_fmac_f32 v231, v227, v230\n"
  "v_mfma_f32_16x16x32_bf16 v[164:167], v[208:211], v[36:39], v[164:167]\n"
  "ds_read_b128 v[208:211], v241 offset:57344\n"
  "buffer_load_dwordx4 v[196:199], v240, s[36:39], s71 offen offset:1024 sc1\n"
  "s_waitcnt lgkmcnt(4)\n"
  "v_mfma_f32_16x16x32_bf16 v[168:171], v[212:215], v[32:35], v[168:171]\n"
  "s_add_u32 m0, s45, 0x16000\n"
  "s_add_u32 s68, s52, 0x5e000\n"
  "buffer_load_dwordx4 v240, s[36:39], s68 offen lds\n"
  "v_fmac_f32 v236, v224, v146\n"
  "v_mfma_f32_16x16x32_bf16 v[172:175], v[212:215], v[36:39], v[172:175]\n"
  "ds_read_b128 v[212:215], v241 offset:58368\n"
  "v_fmac_f32 v237, v224, v154\n"
  "v_fmac_f32 v229, v226, v150\n"
  "s_waitcnt lgkmcnt(4)\n"
  "v_mfma_f32_16x16x32_bf16 v[160:163], v[216:219], v[40:43], v[160:163]\n"
  "v_fmac_f32 v231, v226, v158\n"
  "v_mul_f32 v146, v236, v237\n"
  "v_mul_f32 v150, v229, v231\n"
  "v_mfma_f32_16x16x32_bf16 v[164:167], v[216:219], v[44:47], v[164:167]\n"
  "ds_read_b128 v[216:219], v241 offset:59392\n"
  "buffer_load_dwordx4 v[200:203], v240, s[36:39], s71 offen offset:2048 sc1\n"
  "s_waitcnt lgkmcnt(3)\n"
  "v_mfma_f32_16x16x32_bf16 v[168:171], v[220:223], v[40:43], v[168:171]\n"
  "v_fma_f32 v236, v225, v232, v233\n"
  "v_fma_f32 v237, v225, v234, v235\n"
  "v_fmac_f32 v233, v227, v232\n"
  "v_mfma_f32_16x16x32_bf16 v[172:175], v[220:223], v[44:47], v[172:175]\n"
  "ds_read_b128 v[220:223], v241 offset:60416\n"
  "buffer_load_dwordx4 v[204:207], v240, s[36:39], s71 offen offset:3072 sc1\n"
  "s_waitcnt lgkmcnt(3)\n"
  "v_mfma_f32_16x16x32_bf16 v[160:163], v[208:211], v[48:51], v[160:163]\n"
  "v_fmac_f32 v235, v227, v234\n"
  "v_fmac_f32 v236, v224, v147\n"
  "v_mfma_f32_16x16x32_bf16 v[164:167], v[208:211], v[52:55], v[164:167]\n"
  "ds_read_b128 v[208:211], v241 offset:61440\n"
  "v_fmac_f32 v237, v224, v155\n"
  "s_waitcnt lgkmcnt(3)\n"
  "v_mfma_f32_16x16x32_bf16 v[168:171], v[212:215], v[48:51], v[168:171]\n"
  "s_add_u32 m0, s45, 0x18000\n"
  "s_add_u32 s68, s52, 0x60000\n"
  "buffer_load_dwordx4 v240, s[36:39], s68 offen lds\n"
  "v_fmac_f32 v233, v226, v151\n"
  "v_mfma_f32_16x16x32_bf16 v[172:175], v[212:215], v[52:55], v[172:175]\n"
  "ds_read_b128 v[212:215], v241 offset:62464\n"
  "v_fmac_f32 v235, v226, v159\n"
  "v_mul_f32 v147, v236, v237\n"
  "s_waitcnt lgkmcnt(3)\n"
  "v_mfma_f32_16x16x32_bf16 v[160:163], v[216:219], v[56:59], v[160:163]\n"
  "v_mul_f32 v151, v233, v235\n"
  "v_cvt_pk_bf16_f32 v152, v144, v145\n"
  "v_mfma_f32_16x16x32_bf16 v[164:167], v[216:219], v[60:63], v[164:167]\n"
  "ds_read_b128 v[216:219], v241 offset:63488\n"
  "v_cvt_pk_bf16_f32 v153, v146, v147\n"
  "s_waitcnt lgkmcnt(3)\n"
  "v_mfma_f32_16x16x32_bf16 v[168:171], v[220:223], v[56:59], v[168:171]\n"
  "v_cvt_pk_bf16_f32 v154, v148, v149\n"
  "v_cvt_pk_bf16_f32 v155, v150, v151\n"
  "v_mfma_f32_16x16x32_bf16 v[172:175], v[220:223], v[60:63], v[172:175]\n"
  "ds_read_b128 v[220:223], v241 offset:64512\n"
  "s_waitcnt lgkmcnt(3)\n"
  "v_mfma_f32_16x16x32_bf16 v[160:163], v[208:211], v[64:67], v[160:163]\n"
  "s_add_u32 s69, s48, 0xc000\n"
  "buffer_store_dwordx4 v[152:155], v240, s[36:39], s69 offen sc1\n"
  "v_mfma_f32_16x16x32_bf16 v[164:167], v[208:211], v[68:71], v[164:167]\n"
  "ds_read_b128 v[208:211], v242 offset:0\n"
  "s_waitcnt lgkmcnt(3)\n"
  "v_mfma_f32_16x16x32_bf16 v[168:171], v[212:215], v[64:67], v[168:171]\n"
  "v_mfma_f32_16x16x32_bf16 v[172:175], v[212:215], v[68:71], v[172:175]\n"
  "ds_read_b128 v[212:215], v242 offset:1024\n"
  "s_waitcnt lgkmcnt(3)\n"
  "v_mfma_f32_16x16x32_bf16 v[160:163], v[216:219], v[72:75], v[160:163]\n"
  "v_mfma_f32_16x16x32_bf16 v[164:167], v[216:219], v[76:79], v[164:167]\n"
  "ds_read_b128 v[216:219], v242 offset:2048\n"
  "s_waitcnt lgkmcnt(3)\n"
  "v_mfma_f32_16x16x32_bf16 v[168:171], v[220:223], v[72:75], v[168:171]\n"
  "v_mfma_f32_16x16x32_bf16 v[172:175], v[220:223], v[76:79], v[172:175]\n"
  "ds_read_b128 v[220:223], v242 offset:3072\n"
  "s_waitcnt lgkmcnt(3)\n"
  "v_mfma_f32_16x16x32_bf16 v[160:163], v[208:211], v[80:83], v[160:163]\n"
  "v_mfma_f32_16x16x32_bf16 v[164:167], v[208:211], v[84:87], v[164:167]\n"
  "ds_read_b128 v[208:211], v242 offset:4096\n"
  "s_waitcnt lgkmcnt(3)\n"
  "v_mfma_f32_16x16x32_bf16 v[168:171], v[212:215], v[80:83], v[168:171]\n"
  "v_mfma_f32_16x16x32_bf16 v[172:175], v[212:215], v[84:87], v[172:175]\n"
  "ds_read_b128 v[212:215], v242 offset:5120\n"
  "s_waitcnt lgkmcnt(3)\n"
  "v_mfma_f32_16x16x32_bf16 v[160:163], v[216:219], v[88:91], v[160:163]\n"
  "v_mfma_f32_16x16x32_bf16 v[164:167], v[216:219], v[92:95], v[164:167]\n"
  "ds_read_b128 v[216:219], v242 offset:6144\n"
  "s_waitcnt lgkmcnt(3)\n"
  "v_mfma_f32_16x16x32_bf16 v[168:171], v[220:223], v[88:91], v[168:171]\n"
  "v_mfma_f32_16x16x32_bf16 v[172:175], v[220:223], v[92:95], v[172:175]\n"
  "ds_read_b128 v[220:223], v242 offset:7168\n"
  "s_waitcnt lgkmcnt(3)\n"
  "v_mfma_f32_16x16x32_bf16 v[160:163], v[208:211], v[96:99], v[160:163]\n"
  "v_mfma_f32_16x16x32_bf16 v[164:167], v[208:211], v[100:103], v[164:167]\n"
  "ds_read_b128 v[208:211], v242 offset:8192\n"
  "s_waitcnt lgkmcnt(3)\n"
  "v_mfma_f32_16x16x32_bf16 v[168:171], v[212:215], v[96:99], v[168:171]\n"
  "v_mfma_f32_16x16x32_bf16 v[172:175], v[212:215], v[100:103], v[172:175]\n"
  "ds_read_b128 v[212:215], v242 offset:9216\n"
  "s_waitcnt lgkmcnt(3)\n"
  "v_mfma_f32_16x16x32_bf16 v[160:163], v[216:219], v[104:107], v[160:163]\n"
  "v_mfma_f32_16x16x32_bf16 v[164:167], v[216:219], v[108:111], v[164:167]\n"
  "ds_read_b128 v[216:219], v242 offset:10240\n"
  "s_waitcnt lgkmcnt(3)\n"
  "v_mfma_f32_16x16x32_bf16 v[168:171], v[220:223], v[104:107], v[168:171]\n"
  "v_mfma_f32_16x16x32_bf16 v[172:175], v[220:223], v[108:111], v[172:175]\n"
  "ds_read_b128 v[220:223], v242 offset:11264\n"
  "s_waitcnt lgkmcnt(3)\n"
  "v_mfma_f32_16x16x32_bf16 v[160:163], v[208:211], v[112:115], v[160:163]\n"
  "v_mfma_f32_16x16x32_bf16 v[164:167], v[208:211], v[116:119], v[164:167]\n"
  "ds_read_b128 v[208:211], v242 offset:12288\n"
  "s_waitcnt lgkmcnt(3)\n"
  "v_mfma_f32_16x16x32_bf16 v[168:171], v[212:215], v[112:115], v[168:171]\n"
  "v_mfma_f32_16x16x32_bf16 v[172:175], v[212:215], v[116:119], v[172:175]\n"
  "ds_read_b128 v[212:215], v242 offset:13312\n"
  "s_waitcnt lgkmcnt(3)\n"
  "v_mfma_f32_16x16x32_bf16 v[160:163], v[216:219], v[120:123], v[160:163]\n"
  "v_mfma_f32_16x16x32_bf16 v[164:167], v[216:219], v[124:127], v[164:167]\n"
  "ds_read_b128 v[216:219], v242 offset:14336\n"
  "s_waitcnt lgkmcnt(3)\n"
  "v_mfma_f32_16x16x32_bf16 v[168:171], v[220:223], v[120:123], v[168:171]\n"
  "v_mfma_f32_16x16x32_bf16 v[172:175], v[220:223], v[124:127], v[172:175]\n"
  "ds_read_b128 v[220:223], v242 offset:15360\n"
  "s_waitcnt vmcnt(0)\n"
  "s_barrier\n"
  "s_cmp_eq_u32 s45, 0\n"
  "s_cbranch_scc1 .Lk34_pub284\n"
  ".Lk34_pub284_r:\n"
  "s_waitcnt lgkmcnt(3)\n"
  "v_mfma_f32_16x16x32_bf16 v[160:163], v[208:211], v[128:131], v[160:163]\n"
  "v_mfma_f32_16x16x32_bf16 v[164:167], v[208:211], v[132:135], v[164:167]\n"
  "ds_read_b128 v[208:211], v240 offset:0\n"
  "s_waitcnt lgkmcnt(3)\n"
  "v_mfma_f32_16x16x32_bf16 v[168:171], v[212:215], v[128:131], v[168:171]\n"
  "s_add_u32 m0, s45, 0x1a000\n"
  "s_add_u32 s68, s52, 0x62000\n"
  "buffer_load_dwordx4 v240, s[36:39], s68 offen lds\n"
  "v_mfma_f32_16x16x32_bf16 v[172:175], v[212:215], v[132:135], v[172:175]\n"
  "ds_read_b128 v[212:215], v240 offset:1024\n"
  "s_waitcnt lgkmcnt(3)\n"
  "v_mfma_f32_16x16x32_bf16 v[160:163], v[216:219], v[136:139], v[160:163]\n"
  "ds_read_b128 v[228:231], v244 offset:1792\n"
  "v_mfma_f32_16x16x32_bf16 v[164:167], v[216:219], v[140:143], v[164:167]\n"
  "ds_read_b128 v[216:219], v240 offset:2048\n"
  "s_waitcnt lgkmcnt(4)\n"
  "v_mfma_f32_16x16x32_bf16 v[168:171], v[220:223], v[136:139], v[168:171]\n"
  "s_add_u32 m0, s45, 0x1c000\n"
  "s_add_u32 s68, s52, 0x64000\n"
  "buffer_load_dwordx4 v240, s[36:39], s68 offen lds\n"
  "v_mfma_f32_16x16x32_bf16 v[172:175], v[220:223], v[140:143], v[172:175]\n"
  "ds_read_b128 v[220:223], v240 offset:3072\n"
  "ds_read_b128 v[232:235], v244 offset:1808\n"
  "s_waitcnt lgkmcnt(5)\n"
  "v_mfma_f32_16x16x32_bf16 v[144:147], v[208:211], v[0:3], 0\n"
  "v_mfma_f32_16x16x32_bf16 v[148:151], v[208:211], v[4:7], 0\n"
  "ds_read_b128 v[208:211], v240 offset:4096\n"
  "s_waitcnt lgkmcnt(5)\n"
  "v_mfma_f32_16x16x32_bf16 v[152:155], v[212:215], v[0:3], 0\n"
  "s_add_u32 m0, s45, 0x1e000\n"
  "s_add_u32 s68, s52, 0x66000\n"
  "buffer_load_dwordx4 v240, s[36:39], s68 offen lds\n"
  "s_waitcnt lgkmcnt(4)\n"
  "v_fma_f32 v236, v225, v228, v229\n"
  "v_mfma_f32_16x16x32_bf16 v[156:159], v[212:215], v[4:7], 0\n"
  "ds_read_b128 v[212:215], v240 offset:5120\n"
  "v_fma_f32 v237, v225, v230, v231\n"
  "v_fmac_f32 v229, v227, v228\n"
  "s_waitcnt lgkmcnt(4)\n"
  "v_mfma_f32_16x16x32_bf16 v[144:147], v[216:219], v[8:11], v[144:147]\n"
  "v_fmac_f32 v231, v227, v230\n"
  "v_fmac_f32 v236, v224, v160\n"
  "v_mfma_f32_16x16x32_bf16 v[148:151], v[216:219], v[12:15], v[148:151]\n"
  "ds_read_b128 v[216:219], v240 offset:6144\n"
  "v_fmac_f32 v237, v224, v168\n"
  "s_waitcnt lgkmcnt(4)\n"
  "v_mfma_f32_16x16x32_bf16 v[152:155], v[220:223], v[8:11], v[152:155]\n"
  "s_add_u32 m0, s45, 0x20000\n"
  "s_add_u32 s68, s52, 0x68000\n"
  "buffer_load_dwordx4 v240, s[36:39], s68 offen lds\n"
  "v_fmac_f32 v229, v226, v164\n"
  "v_mfma_f32_16x16x32_bf16 v[156:159], v[220:223], v[12:15], v[156:159]\n"
  "ds_read_b128 v[220:223], v240 offset:7168\n"
  "v_fmac_f32 v231, v226, v172\n"
  "v_mul_f32 v160, v236, v237\n"
  "s_waitcnt lgkmcnt(3)\n"
  "v_mfma_f32_16x16x32_bf16 v[144:147], v[208:211], v[16:19], v[144:147]\n"
  "v_mul_f32 v164, v229, v231\n"
  "ds_read_b128 v[228:231], v244 offset:1824\n"
  "v_fma_f32 v236, v225, v232, v233\n"
  "v_fma_f32 v237, v225, v234, v235\n"
  "v_mfma_f32_16x16x32_bf16 v[148:151], v[208:211], v[20:23], v[148:151]\n"
  "ds_read_b128 v[208:211], v240 offset:8192\n"
  "s_add_u32 s71, s56, 0x4000\n"
  "buffer_load_dwordx4 v[0:3], v240, s[36:39], s71 offen sc1\n"
  "s_waitcnt lgkmcnt(4)\n"
  "v_mfma_f32_16x16x32_bf16 v[152:155], v[212:215], v[16:19], v[152:155]\n"
  "v_fmac_f32 v233, v227, v232\n"
  "v_fmac_f32 v235, v227, v234\n"
  "v_mfma_f32_16x16x32_bf16 v[156:159], v[212:215], v[20:23], v[156:159]\n"
  "ds_read_b128 v[212:215], v240 offset:9216\n"
  "v_fmac_f32 v236, v224, v161\n"
  "s_waitcnt lgkmcnt(4)\n"
  "v_mfma_f32_16x16x32_bf16 v[144:147], v[216:219], v[24:27], v[144:147]\n"
  "v_fmac_f32 v237, v224, v169\n"
  "v_fmac_f32 v233, v226, v165\n"
  "v_fmac_f32 v235, v226, v173\n"
  "v_mfma_f32_16x16x32_bf16 v[148:151], v[216:219], v[28:31], v[148:151]\n"
  "ds_read_b128 v[216:219], v240 offset:10240\n"
  "buffer_load_dwordx4 v[4:7], v240, s[36:39], s71 offen offset:1024 sc1\n"
  "s_waitcnt lgkmcnt(4)\n"
  "v_mfma_f32_16x16x32_bf16 v[152:155], v[220:223], v[24:27], v[152:155]\n"
  "v_mul_f32 v161, v236, v237\n"
  "v_mul_f32 v165, v233, v235\n"
  "v_mfma_f32_16x16x32_bf16 v[156:159], v[220:223], v[28:31], v[156:159]\n"
  "ds_read_b128 v[220:223], v240 offset:11264\n"
  "ds_read_b128 v[232:235], v244 offset:1840\n"
  "s_waitcnt lgkmcnt(5)\n"
  "v_fma_f32 v236, v225, v228, v229\n"
  "s_waitcnt lgkmcnt(4)\n"
  "v_mfma_f32_16x16x32_bf16 v[144:147], v[208:211], v[32:35], v[144:147]\n"
  "v_fma_f32 v237, v225, v230, v231\n"
  "v_fmac_f32 v229, v227, v228\n"
  "v_fmac_f32 v231, v227, v230\n"
  "v_mfma_f32_16x16x32_bf16 v[148:151], v[208:211], v[36:39], v[148:151]\n"
  "ds_read_b128 v[208:211], v240 offset:12288\n"
  "buffer_load_dwordx4 v[8:11], v240, s[36:39], s71 offen offset:2048 sc1\n"
  "s_waitcnt lgkmcnt(4)\n"
  "v_mfma_f32_16x16x32_bf16 v[152:155], v[212:215], v[32:35], v[152:155]\n"
  "v_fmac_f32 v236, v224, v162\n"
  "v_fmac_f32 v237, v224, v170\n"
  "v_mfma_f32_16x16x32_bf16 v[156:159], v[212:215], v[36:39], v[156:159]\n"
  "ds_read_b128 v[212:215], v240 offset:13312\n"
  "v_fmac_f32 v229, v226, v166\n"
  "s_waitcnt lgkmcnt(4)\n"
  "v_mfma_f32_16x16x32_bf16 v[144:147], v[216:219], v[40:43], v[144:147]\n"
  "v_fmac_f32 v231, v226, v174\n"
  "v_mul_f32 v162, v236, v237\n"
  "v_mul_f32 v166, v229, v231\n"
  "v_mfma_f32_16x16x32_bf16 v[148:151], v[216:219], v[44:47], v[148:151]\n"
  "ds_read_b128 v[216:219], v240 offset:14336\n"
  "buffer_load_dwordx4 v[12:15], v240, s[36:39], s71 offen offset:3072 sc1\n"
  "s_waitcnt lgkmcnt(4)\n"
  "v_mfma_f32_16x16x32_bf16 v[152:155], v[220:223], v[40:43], v[152:155]\n"
  "s_waitcnt lgkmcnt(3)\n"
  "v_fma_f32 v236, v225, v232, v233\n"
  "v_fma_f32 v237, v225, v234, v235\n"
  "v_mfma_f32_16x16x32_bf16 v[156:159], v[220:223], v[44:47], v[156:159]\n"
  "ds_read_b128 v[220:223], v240 offset:15360\n"
  "v_fmac_f32 v233, v227, v232\n"
  "s_waitcnt lgkmcnt(3)\n"
  "v_mfma_f32_16x16x32_bf16 v[144:147], v[208:211], v[48:51], v[144:147]\n"
  "v_fmac_f32 v235, v227, v234\n"
  "v_fmac_f32 v236, v224, v163\n"
  "v_mfma_f32_16x16x32_bf16 v[148:151], v[208:211], v[52:55], v[148:151]\n"
  "ds_read_b128 v[208:211], v240 offset:16384\n"
  "v_fmac_f32 v237, v224, v171\n"
  "s_waitcnt lgkmcnt(3)\n"
  "v_mfma_f32_16x16x32_bf16 v[152:155], v[212:215], v[48:51], v[152:155]\n"
  "v_fmac_f32 v233, v226, v167\n"
  "v_fmac_f32 v235, v226, v175\n"
  "v_mfma_f32_16x16x32_bf16 v[156:159], v[212:215], v[52:55], v[156:159]\n"
  "ds_read_b128 v[212:215], v240 offset:17408\n"
  "v_mul_f32 v163, v236, v237\n"
  "s_waitcnt lgkmcnt(3)\n"
  "v_mfma_f32_16x16x32_bf16 v[144:147], v[216:219], v[56:59], v[144:147]\n"
  "v_mul_f32 v167, v233, v235\n"
  "v_cvt_pk_bf16_f32 v168, v160, v161\n"
  "v_cvt_pk_bf16_f32 v169, v162, v163\n"
  "v_mfma_f32_16x16x32_bf16 v[148:151], v[216:219], v[60:63], v[148:151]\n"
  "ds_read_b128 v[216:219], v240 offset:18432\n"
  "s_add_u32 s71, s56, 0x6000\n"
  "buffer_load_dwordx4 v[16:19], v240, s[36:39], s71 offen sc1\n"
  "s_waitcnt lgkmcnt(3)\n"
  "v_mfma_f32_16x16x32_bf16 v[152:155], v[220:223], v[56:59], v[152:155]\n"
  "v_cvt_pk_bf16_f32 v170, v164, v165\n"
  "v_cvt_pk_bf16_f32 v171, v166, v167\n"
  "v_mfma_f32_16x16x32_bf16 v[156:159], v[220:223], v[60:63], v[156:159]\n"
  "ds_read_b128 v[220:223], v240 offset:19456\n"
  "s_waitcnt lgkmcnt(3)\n"
  "v_mfma_f32_16x16x32_bf16 v[144:147], v[208:211], v[64:67], v[144:147]\n"
  "s_add_u32 s69, s48, 0xe000\n"
  "buffer_store_dwordx4 v[168:171], v240, s[36:39], s69 offen sc1\n"
  "v_mfma_f32_16x16x32_bf16 v[148:151], v[208:211], v[68:71], v[148:151]\n"
  "ds_read_b128 v[208:211], v240 offset:20480\n"
  "s_waitcnt lgkmcnt(3)\n"
  "v_mfma_f32_16x16x32_bf16 v[152:155], v[212:215], v[64:67], v[152:155]\n"
  "buffer_load_dwordx4 v[20:23], v240, s[36:39], s71 offen offset:1024 sc1\n"
  "v_mfma_f32_16x16x32_bf16 v[156:159], v[212:215], v[68:71], v[156:159]\n"
  "ds_read_b128 v[212:215], v240 offset:21504\n"
  "s_waitcnt lgkmcnt(3)\n"
  "v_mfma_f32_16x16x32_bf16 v[144:147], v[216:219], v[72:75], v[144:147]\n"
  "v_mfma_f32_16x16x32_bf16 v[148:151], v[216:219], v[76:79], v[148:151]\n"
  "ds_read_b128 v[216:219], v240 offset:22528\n"
  "s_waitcnt lgkmcnt(3)\n"
  "v_mfma_f32_16x16x32_bf16 v[152:155], v[220:223], v[72:75], v[152:155]\n"
  "buffer_load_dwordx4 v[24:27], v240, s[36:39], s71 offen offset:2048 sc1\n"
  "v_mfma_f32_16x16x32_bf16 v[156:159], v[220:223], v[76:79], v[156:159]\n"
  "ds_read_b128 v[220:223], v240 offset:23552\n"
  "s_waitcnt lgkmcnt(3)\n"
  "v_mfma_f32_16x16x32_bf16 v[144:147], v[208:211], v[80:83], v[144:147]\n"
  "v_mfma_f32_16x16x32_bf16 v[148:151], v[208:211], v[84:87], v[148:151]\n"
  "ds_read_b128 v[208:211], v240 offset:24576\n"
  "s_waitcnt lgkmcnt(3)\n"
  "v_mfma_f32_16x16x32_bf16 v[152:155], v[212:215], v[80:83], v[152:155]\n"
  "buffer_load_dwordx4 v[28:31], v240, s[36:39], s71 offen offset:3072 sc1\n"
  "v_mfma_f32_16x16x32_bf16 v[156:159], v[212:215], v[84:87], v[156:159]\n"
  "ds_read_b128 v[212:215], v240 offset:25600\n"
  "s_waitcnt lgkmcnt(3)\n"
  "v_mfma_f32_16x16x32_bf16 v[144:147], v[216:219], v[88:91], v[144:147]\n"
  "v_mfma_f32_16x16x32_bf16 v[148:151], v[216:219], v[92:95], v[148:151]\n"
  "ds_read_b128 v[216:219], v240 offset:26624\n"
  "s_waitcnt lgkmcnt(3)\n"
  "v_mfma_f32_16x16x32_bf16 v[152:155], v[220:223], v[88:91], v[152:155]\n"
  "v_mfma_f32_16x16x32_bf16 v[156:159], v[220:223], v[92:95], v[156:159]\n"
  "ds_read_b128 v[220:223], v240 offset:27648\n"
  "s_waitcnt lgkmcnt(3)\n"
  "v_mfma_f32_16x16x32_bf16 v[144:147], v[208:211], v[96:99], v[144:147]\n"
  "buffer_load_dword v249, v250, s[36:39], s55 offen sc1\n"
  "v_mfma_f32_16x16x32_bf16 v[148:151], v[208:211], v[100:103], v[148:151]\n"
  "ds_read_b128 v[208:211], v240 offset:28672\n"
  "s_waitcnt lgkmcnt(3)\n"
  "v_mfma_f32_16x16x32_bf16 v[152:155], v[212:215], v[96:99], v[152:155]\n"
  "v_mfma_f32_16x16x32_bf16 v[156:159], v[212:215], v[100:103], v[156:159]\n"
  "ds_read_b128 v[212:215], v240 offset:29696\n"
  "s_waitcnt lgkmcnt(3)\n"
  "v_mfma_f32_16x16x32_bf16 v[144:147], v[216:219], v[104:107], v[144:147]\n"
  "s_add_u32 s71, s56, 0x8000\n"
  "buffer_load_dwordx4 v[32:35], v240, s[36:39], s71 offen sc1\n"
  "v_mfma_f32_16x16x32_bf16 v[148:151], v[216:219], v[108:111], v[148:151]\n"
  "ds_read_b128 v[216:219], v240 offset:30720\n"
  "s_waitcnt lgkmcnt(3)\n"
  "v_mfma_f32_16x16x32_bf16 v[152:155], v[220:223], v[104:107], v[152:155]\n"
  "v_mfma_f32_16x16x32_bf16 v[156:159], v[220:223], v[108:111], v[156:159]\n"
  "ds_read_b128 v[220:223], v240 offset:31744\n"
  "s_waitcnt lgkmcnt(3)\n"
  "v_mfma_f32_16x16x32_bf16 v[144:147], v[208:211], v[112:115], v[144:147]\n"
  "buffer_load_dwordx4 v[36:39], v240, s[36:39], s71 offen offset:1024 sc1\n"
  "v_mfma_f32_16x16x32_bf16 v[148:151], v[208:211], v[116:119], v[148:151]\n"
  "ds_read_b128 v[208:211], v240 offset:32768\n"
  "s_waitcnt lgkmcnt(3)\n"
  "v_mfma_f32_16x16x32_bf16 v[152:155], v[212:215], v[112:115], v[152:155]\n"
  "v_mfma_f32_16x16x32_bf16 v[156:159], v[212:215], v[116:119], v[156:159]\n"
  "ds_read_b128 v[212:215], v240 offset:33792\n"
  "s_waitcnt lgkmcnt(3)\n"
  "v_mfma_f32_16x16x32_bf16 v[144:147], v[216:219], v[120:123], v[144:147]\n"
  "buffer_load_dwordx4 v[40:43], v240, s[36:39], s71 offen offset:2048 sc1\n"
  "v_mfma_f32_16x16x32_bf16 v[148:151], v[216:219], v[124:127], v[148:151]\n"
  "ds_read_b128 v[216:219], v240 offset:34816\n"
  "s_waitcnt lgkmcnt(3)\n"
  "v_mfma_f32_16x16x32_bf16 v[152:155], v[220:223], v[120:123], v[152:155]\n"
  "v_mfma_f32_16x16x32_bf16 v[156:159], v[220:223], v[124:127], v[156:159]\n"
  "ds_read_b128 v[220:223], v240 offset:35840\n"
  "s_waitcnt lgkmcnt(3)\n"
  "v_mfma_f32_16x16x32_bf16 v[144:147], v[208:211], v[128:131], v[144:147]\n"
  "buffer_load_dwordx4 v[44:47], v240, s[36:39], s71 offen offset:3072 sc1\n"
  "v_mfma_f32_16x16x32_bf16 v[148:151], v[208:211], v[132:135], v[148:151]\n"
  "ds_read_b128 v[208:211], v246 offset:36864\n"
  "s_waitcnt lgkmcnt(3)\n"
  "v_mfma_f32_16x16x32_bf16 v[152:155], v[212:215], v[128:131], v[152:155]\n"
  "v_mfma_f32_16x16x32_bf16 v[156:159], v[212:215], v[132:135], v[156:159]\n"
  "ds_read_b128 v[212:215], v246 offset:38912\n"
  "s_waitcnt lgkmcnt(3)\n"
  "v_mfma_f32_16x16x32_bf16 v[144:147], v[216:219], v[136:139], v[144:147]\n"
  "ds_read_b128 v[228:231], v244 offset:2048\n"
  "v_mfma_f32_16x16x32_bf16 v[148:151], v[216:219], v[140:143], v[148:151]\n"
  "ds_read_b128 v[216:219], v246 offset:40960\n"
  "s_waitcnt lgkmcnt(4)\n"
  "v_mfma_f32_16x16x32_bf16 v[152:155], v[220:223], v[136:139], v[152:155]\n"
  "v_swap_b32 v178, v180\n"
  "v_swap_b32 v179, v181\n"
  "v_mfma_f32_16x16x32_bf16 v[156:159], v[220:223], v[140:143], v[156:159]\n"
  "ds_read_b128 v[220:223], v246 offset:43008\n"
  "ds_read_b128 v[232:235], v244 offset:2064\n"
  "s_waitcnt lgkmcnt(5)\n"
  "v_mfma_f32_16x16x32_bf16 v[136:139], v[208:211], v[176:179], 0\n"
  "v_mfma_f32_16x16x32_bf16 v[140:143], v[208:211], v[180:183], 0\n"
  "ds_read_b128 v[208:211], v246 offset:45056\n"
  "s_waitcnt lgkmcnt(5)\n"
  "v_mfma_f32_16x16x32_bf16 v[96:99], v[212:215], v[176:179], 0\n"
  "s_waitcnt lgkmcnt(4)\n"
  "v_fma_f32 v236, v225, v228, v229\n"
  "v_fma_f32 v237, v225, v230, v231\n"
  "v_mfma_f32_16x16x32_bf16 v[100:103], v[212:215], v[180:183], 0\n"
  "ds_read_b128 v[212:215], v246 offset:47104\n"
  "v_fmac_f32 v229, v227, v228\n"
  "v_fmac_f32 v231, v227, v230\n"
  "s_waitcnt lgkmcnt(4)\n"
  "v_mfma_f32_16x16x32_bf16 v[112:115], v[216:219], v[176:179], 0\n"
  "v_fmac_f32 v236, v224, v144\n"
  "v_fmac_f32 v237, v224, v152\n"
  "v_mfma_f32_16x16x32_bf16 v[116:119], v[216:219], v[180:183], 0\n"
  "ds_read_b128 v[216:219], v246 offset:49152\n"
  "v_fmac_f32 v229, v226, v148\n"
  "v_fmac_f32 v231, v226, v156\n"
  "s_waitcnt lgkmcnt(4)\n"
  "v_mfma_f32_16x16x32_bf16 v[128:131], v[220:223], v[176:179], 0\n"
  "v_mul_f32 v144, v236, v237\n"
  "v_mul_f32 v148, v229, v231\n"
  "ds_read_b128 v[228:231], v244 offset:2080\n"
  "v_mfma_f32_16x16x32_bf16 v[132:135], v[220:223], v[180:183], 0\n"
  "ds_read_b128 v[220:223], v246 offset:51200\n"
  "s_waitcnt lgkmcnt(5)\n"
  "v_fma_f32 v236, v225, v232, v233\n"
  "v_fma_f32 v237, v225, v234, v235\n"
  "s_waitcnt lgkmcnt(4)\n"
  "v_mfma_f32_16x16x32_bf16 v[88:91], v[208:211], v[176:179], 0\n"
  "v_fmac_f32 v233, v227, v232\n"
  "v_fmac_f32 v235, v227, v234\n"
  "v_mfma_f32_16x16x32_bf16 v[92:95], v[208:211], v[180:183], 0\n"
  "ds_read_b128 v[208:211], v246 offset:53248\n"
  "v_fmac_f32 v236, v224, v145\n"
  "v_fmac_f32 v237, v224, v153\n"
  "s_waitcnt lgkmcnt(4)\n"
  "v_mfma_f32_16x16x32_bf16 v[104:107], v[212:215], v[176:179], 0\n"
  "v_fmac_f32 v233, v226, v149\n"
  "v_fmac_f32 v235, v226, v157\n"
  "v_mfma_f32_16x16x32_bf16 v[108:111], v[212:215], v[180:183], 0\n"
  "ds_read_b128 v[212:215], v246 offset:55296\n"
  "v_mul_f32 v145, v236, v237\n"
  "v_mul_f32 v149, v233, v235\n"
  "ds_read_b128 v[232:235], v244 offset:2096\n"
  "s_waitcnt lgkmcnt(5)\n"
  "v_mfma_f32_16x16x32_bf16 v[120:123], v[216:219], v[176:179], 0\n"
  "v_swap_b32 v186, v188\n"
  "v_swap_b32 v187, v189\n"
  "s_waitcnt lgkmcnt(4)\n"
  "v_fma_f32 v236, v225, v228, v229\n"
  "v_mfma_f32_16x16x32_bf16 v[124:127], v[216:219], v[180:183], 0\n"
  "ds_read_b128 v[216:219], v246 offset:57344\n"
  "v_fma_f32 v237, v225, v230, v231\n"
  "v_fmac_f32 v229, v227, v228\n"
  "v_fmac_f32 v231, v227, v230\n"
  "s_waitcnt lgkmcnt(4)\n"
  "v_mfma_f32_16x16x32_bf16 v[136:139], v[220:223], v[184:187], v[136:139]\n"
  "v_fmac_f32 v236, v224, v146\n"
  "v_fmac_f32 v237, v224, v154\n"
  "v_fmac_f32 v229, v226, v150\n"
  "v_mfma_f32_16x16x32_bf16 v[140:143], v[220:223], v[188:191], v[140:143]\n"
  "ds_read_b128 v[220:223], v246 offset:59392\n"
  "v_fmac_f32 v231, v226, v158\n"
  "s_waitcnt vmcnt(4)\n"
  "v_cmp_gt_u32 vcc, 7, v249\n"
  "s_cbranch_vccnz .Lk34_fl7\n"
  ".Lk34_fl7_r:\n"
  "s_barrier\n"
  "s_waitcnt lgkmcnt(4)\n"
  "v_mfma_f32_16x16x32_bf16 v[96:99], v[208:211], v[184:187], v[96:99]\n"
  "v_mul_f32 v146, v236, v237\n"
  "v_mul_f32 v150, v229, v231\n"
  "v_mfma_f32_16x16x32_bf16 v[100:103], v[208:211], v[188:191], v[100:103]\n"
  "ds_read_b128 v[208:211], v246 offset:61440\n"
  "s_waitcnt lgkmcnt(3)\n"
  "v_fma_f32 v236, v225, v232, v233\n"
  "v_fma_f32 v237, v225, v234, v235\n"
  "v_mfma_f32_16x16x32_bf16 v[112:115], v[212:215], v[184:187], v[112:115]\n"
  "s_add_u32 m0, s45, 0x22000\n"
  "s_add_u32 s68, s52, 0x6a000\n"
  "buffer_load_dwordx4 v240, s[36:39], s68 offen lds\n"
  "v_fmac_f32 v233, v227, v232\n"
  "v_mfma_f32_16x16x32_bf16 v[116:119], v[212:215], v[188:191], v[116:119]\n"
  "ds_read_b128 v[212:215], v246 offset:63488\n"
  "v_fmac_f32 v235, v227, v234\n"
  "v_fmac_f32 v236, v224, v147\n"
  "v_fmac_f32 v237, v224, v155\n"
  "s_waitcnt lgkmcnt(3)\n"
  "v_mfma_f32_16x16x32_bf16 v[128:131], v[216:219], v[184:187], v[128:131]\n"
  "v_fmac_f32 v233, v226, v151\n"
  "v_fmac_f32 v235, v226, v159\n"
  "v_mul_f32 v147, v236, v237\n"
  "v_mfma_f32_16x16x32_bf16 v[132:135], v[216:219], v[188:191], v[132:135]\n"
  "ds_read_b128 v[216:219], v247 offset:0\n"
  "v_mul_f32 v151, v233, v235\n"
  "s_add_u32 s71, s56, 0xa000\n"
  "buffer_load_dwordx4 v[48:51], v240, s[36:39], s71 offen sc1\n"
  "s_waitcnt lgkmcnt(3)\n"
  "v_mfma_f32_16x16x32_bf16 v[88:91], v[220:223], v[184:187], v[88:91]\n"
  "s_add_u32 m0, s45, 0\n"
  "s_add_u32 s68, s52, 0x6c000\n"
  "buffer_load_dwordx4 v240, s[36:39], s68 offen lds\n"
  "v_cvt_pk_bf16_f32 v152, v144, v145\n"
  "v_mfma_f32_16x16x32_bf16 v[92:95], v[220:223], v[188:191], v[92:95]\n"
  "ds_read_b128 v[220:223], v247 offset:2048\n"
  "v_cvt_pk_bf16_f32 v153, v146, v147\n"
  "v_cvt_pk_bf16_f32 v154, v148, v149\n"
  "v_cvt_pk_bf16_f32 v155, v150, v151\n"
  "s_waitcnt lgkmcnt(3)\n"
  "v_mfma_f32_16x16x32_bf16 v[104:107], v[208:211], v[184:187], v[104:107]\n"
  "s_add_u32 s69, s48, 0x10000\n"
  "buffer_store_dwordx4 v[152:155], v240, s[36:39], s69 offen sc1\n"
  "v_mfma_f32_16x16x32_bf16 v[108:111], v[208:211], v[188:191], v[108:111]\n"
  "ds_read_b128 v[208:211], v247 offset:4096\n"
  "s_waitcnt lgkmcnt(3)\n"
  "v_mfma_f32_16x16x32_bf16 v[120:123], v[212:215], v[184:187], v[120:123]\n"
  "v_swap_b32 v194, v196\n"
  "v_swap_b32 v195, v197\n"
  "v_mfma_f32_16x16x32_bf16 v[124:127], v[212:215], v[188:191], v[124:127]\n"
  "ds_read_b128 v[212:215], v247 offset:6144\n"
  "s_add_u32 m0, s45, 0x2000\n"
  "s_add_u32 s68, s52, 0x6e000\n"
  "buffer_load_dwordx4 v240, s[36:39], s68 offen lds\n"
  "s_waitcnt lgkmcnt(3)\n"
  "v_mfma_f32_16x16x32_bf16 v[136:139], v[216:219], v[192:195], v[136:139]\n"
  "buffer_load_dwordx4 v[52:55], v240, s[36:39], s71 offen offset:1024 sc1\n"
  "v_mfma_f32_16x16x32_bf16 v[140:143], v[216:219], v[196:199], v[140:143]\n"
  "ds_read_b128 v[216:219], v247 offset:8192\n"
  "s_waitcnt lgkmcnt(3)\n"
  "v_mfma_f32_16x16x32_bf16 v[96:99], v[220:223], v[192:195], v[96:99]\n"
  "s_add_u32 m0, s45, 0x4000\n"
  "s_add_u32 s68, s52, 0x70000\n"
  "buffer_load_dwordx4 v240, s[36:39], s68 offen lds\n"
  "v_mfma_f32_16x16x32_bf16 v[100:103], v[220:223], v[196:199], v[100:103]\n"
  "ds_read_b128 v[220:223], v247 offset:10240\n"
  "s_waitcnt lgkmcnt(3)\n"
  "v_mfma_f32_16x16x32_bf16 v[112:115], v[208:211], v[192:195], v[112:115]\n"
  "buffer_load_dwordx4 v[56:59], v240, s[36:39], s71 offen offset:2048 sc1\n"
  "v_mfma_f32_16x16x32_bf16 v[116:119], v[208:211], v[196:199], v[116:119]\n"
  "ds_read_b128 v[208:211], v247 offset:12288\n"
  "s_waitcnt lgkmcnt(3)\n"
  "v_mfma_f32_16x16x32_bf16 v[128:131], v[212:215], v[192:195], v[128:131]\n"
  "s_add_u32 m0, s45, 0x6000\n"
  "s_add_u32 s68, s52, 0x72000\n"
  "buffer_load_dwordx4 v240, s[36:39], s68 offen lds\n"
  "v_mfma_f32_16x16x32_bf16 v[132:135], v[212:215], v[196:199], v[132:135]\n"
  "ds_read_b128 v[212:215], v247 offset:14336\n"
  "s_waitcnt lgkmcnt(3)\n"
  "v_mfma_f32_16x16x32_bf16 v[88:91], v[216:219], v[192:195], v[88:91]\n"
  "buffer_load_dwordx4 v[60:63], v240, s[36:39], s71 offen offset:3072 sc1\n"
  "v_mfma_f32_16x16x32_bf16 v[92:95], v[216:219], v[196:199], v[92:95]\n"
  "ds_read_b128 v[216:219], v247 offset:16384\n"
  "s_waitcnt lgkmcnt(3)\n"
  "v_mfma_f32_16x16x32_bf16 v[104:107], v[220:223], v[192:195], v[104:107]\n"
  "s_add_u32 m0, s45, 0x8000\n"
  "s_add_u32 s68, s52, 0x74000\n"
  "buffer_load_dwordx4 v240, s[36:39], s68 offen lds\n"
  "v_mfma_f32_16x16x32_bf16 v[108:111], v[220:223], v[196:199], v[108:111]\n"
  "ds_read_b128 v[220:223], v247 offset:18432\n"
  "s_waitcnt lgkmcnt(3)\n"
  "v_mfma_f32_16x16x32_bf16 v[120:123], v[208:211], v[192:195], v[120:123]\n"
  "v_swap_b32 v202, v204\n"
  "v_swap_b32 v203, v205\n"
  "v_mfma_f32_16x16x32_bf16 v[124:127], v[208:211], v[196:199], v[124:127]\n"
  "ds_read_b128 v[208:211], v247 offset:20480\n"
  "s_waitcnt lgkmcnt(3)\n"
  "v_mfma_f32_16x16x32_bf16 v[136:139], v[212:215], v[200:203], v[136:139]\n"
  "s_add_u32 m0, s45, 0xa000\n"
  "s_add_u32 s68, s52, 0x76000\n"
  "buffer_load_dwordx4 v240, s[36:39], s68 offen lds\n"
  "v_mfma_f32_16x16x32_bf16 v[140:143], v[212:215], v[204:207], v[140:143]\n"
  "ds_read_b128 v[212:215], v247 offset:22528\n"
  "s_waitcnt lgkmcnt(3)\n"
  "v_mfma_f32_16x16x32_bf16 v[96:99], v[216:219], v[200:203], v[96:99]\n"
  "v_mfma_f32_16x16x32_bf16 v[100:103], v[216:219], v[204:207], v[100:103]\n"
  "ds_read_b128 v[216:219], v247 offset:24576\n"
  "s_waitcnt lgkmcnt(3)\n"
  "v_mfma_f32_16x16x32_bf16 v[112:115], v[220:223], v[200:203], v[112:115]\n"
  "v_mfma_f32_16x16x32_bf16 v[116:119], v[220:223], v[204:207], v[116:119]\n"
  "ds_read_b128 v[220:223], v247 offset:26624\n"
  "s_waitcnt lgkmcnt(3)\n"
  "v_mfma_f32_16x16x32_bf16 v[128:131], v[208:211], v[200:203], v[128:131]\n"
  "v_mfma_f32_16x16x32_bf16 v[132:135], v[208:211], v[204:207], v[132:135]\n"
  "ds_read_b128 v[208:211], v247 offset:28672\n"
  "s_waitcnt lgkmcnt(3)\n"
  "v_mfma_f32_16x16x32_bf16 v[88:91], v[212:215], v[200:203], v[88:91]\n"
  "v_mfma_f32_16x16x32_bf16 v[92:95], v[212:215], v[204:207], v[92:95]\n"
  "ds_read_b128 v[212:215], v247 offset:30720\n"
  "s_waitcnt lgkmcnt(3)\n"
  "v_mfma_f32_16x16x32_bf16 v[104:107], v[216:219], v[200:203], v[104:107]\n"
  "v_mfma_f32_16x16x32_bf16 v[108:111], v[216:219], v[204:207], v[108:111]\n"
  "ds_read_b128 v[216:219], v247 offset:32768\n"
  "s_waitcnt lgkmcnt(3)\n"
  "v_mfma_f32_16x16x32_bf16 v[120:123], v[220:223], v[200:203], v[120:123]\n"
  "v_swap_b32 v2, v4\n"
  "v_swap_b32 v3, v5\n"
  "v_mfma_f32_16x16x32_bf16 v[124:127], v[220:223], v[204:207], v[124:127]\n"
  "ds_read_b128 v[220:223], v247 offset:34816\n"
  "s_waitcnt lgkmcnt(3)\n"
  "v_mfma_f32_16x16x32_bf16 v[136:139], v[208:211], v[0:3], v[136:139]\n"
  "v_mfma_f32_16x16x32_bf16 v[140:143], v[208:211], v[4:7], v[140:143]\n"
  "ds_read_b128 v[208:211], v247 offset:36864\n"
  "s_waitcnt lgkmcnt(3)\n"
  "v_mfma_f32_16x16x32_bf16 v[96:99], v[212:215], v[0:3], v[96:99]\n"
  "v_mfma_f32_16x16x32_bf16 v[100:103], v[212:215], v[4:7], v[100:103]\n"
  "ds_read_b128 v[212:215], v247 offset:38912\n"
  "s_waitcnt lgkmcnt(3)\n"
  "v_mfma_f32_16x16x32_bf16 v[112:115], v[216:219], v[0:3], v[112:115]\n"
  "v_mfma_f32_16x16x32_bf16 v[116:119], v[216:219], v[4:7], v[116:119]\n"
  "ds_read_b128 v[216:219], v247 offset:40960\n"
  "s_waitcnt lgkmcnt(3)\n"
  "v_mfma_f32_16x16x32_bf16 v[128:131], v[220:223], v[0:3], v[128:131]\n"
  "v_mfma_f32_16x16x32_bf16 v[132:135], v[220:223], v[4:7], v[132:135]\n"
  "ds_read_b128 v[220:223], v247 offset:43008\n"
  "s_waitcnt lgkmcnt(3)\n"
  "v_mfma_f32_16x16x32_bf16 v[88:91], v[208:211], v[0:3], v[88:91]\n"
  "v_mfma_f32_16x16x32_bf16 v[92:95], v[208:211], v[4:7], v[92:95]\n"
  "ds_read_b128 v[208:211], v247 offset:45056\n"
  "s_waitcnt lgkmcnt(3)\n"
  "v_mfma_f32_16x16x32_bf16 v[104:107], v[212:215], v[0:3], v[104:107]\n"
  "v_mfma_f32_16x16x32_bf16 v[108:111], v[212:215], v[4:7], v[108:111]\n"
  "ds_read_b128 v[212:215], v247 offset:47104\n"
  "s_waitcnt lgkmcnt(3)\n"
  "v_mfma_f32_16x16x32_bf16 v[120:123], v[216:219], v[0:3], v[120:123]\n"
  "v_swap_b32 v10, v12\n"
  "v_swap_b32 v11, v13\n"
  "v_mfma_f32_16x16x32_bf16 v[124:127], v[216:219], v[4:7], v[124:127]\n"
  "ds_read_b128 v[216:219], v247 offset:49152\n"
  "s_waitcnt lgkmcnt(3)\n"
  "v_mfma_f32_16x16x32_bf16 v[136:139], v[220:223], v[8:11], v[136:139]\n"
  "v_mfma_f32_16x16x32_bf16 v[140:143], v[220:223], v[12:15], v[140:143]\n"
  "ds_read_b128 v[220:223], v247 offset:51200\n"
  "s_waitcnt lgkmcnt(3)\n"
  "v_mfma_f32_16x16x32_bf16 v[96:99], v[208:211], v[8:11], v[96:99]\n"
  "v_mfma_f32_16x16x32_bf16 v[100:103], v[208:211], v[12:15], v[100:103]\n"
  "ds_read_b128 v[208:211], v247 offset:53248\n"
  "s_waitcnt lgkmcnt(3)\n"
  "v_mfma_f32_16x16x32_bf16 v[112:115], v[212:215], v[8:11], v[112:115]\n"
  "v_mfma_f32_16x16x32_bf16 v[116:119], v[212:215], v[12:15], v[116:119]\n"
  "ds_read_b128 v[212:215], v247 offset:55296\n"
  "s_waitcnt lgkmcnt(3)\n"
  "v_mfma_f32_16x16x32_bf16 v[128:131], v[216:219], v[8:11], v[128:131]\n"
  "v_mfma_f32_16x16x32_bf16 v[132:135], v[216:219], v[12:15], v[132:135]\n"
  "ds_read_b128 v[216:219], v247 offset:57344\n"
  "s_waitcnt lgkmcnt(3)\n"
  "v_mfma_f32_16x16x32_bf16 v[88:91], v[220:223], v[8:11], v[88:91]\n"
  "v_mfma_f32_16x16x32_bf16 v[92:95], v[220:223], v[12:15], v[92:95]\n"
  "ds_read_b128 v[220:223], v247 offset:59392\n"
  "s_waitcnt vmcnt(0)\n"
  "s_barrier\n"
  "s_cmp_eq_u32 s45, 0\n"
  "s_cbranch_scc1 .Lk34_pub364\n"
  ".Lk34_pub364_r:\n"
  "s_waitcnt lgkmcnt(3)\n"
  "v_mfma_f32_16x16x32_bf16 v[104:107], v[208:211], v[8:11], v[104:107]\n"
  "v_mfma_f32_16x16x32_bf16 v[108:111], v[208:211], v[12:15], v[108:111]\n"
  "ds_read_b128 v[208:211], v247 offset:61440\n"
  "s_waitcnt lgkmcnt(3)\n"
  "v_mfma_f32_16x16x32_bf16 v[120:123], v[212:215], v[8:11], v[120:123]\n"
  "v_swap_b32 v18, v20\n"
  "v_swap_b32 v19, v21\n"
  "v_mfma_f32_16x16x32_bf16 v[124:127], v[212:215], v[12:15], v[124:127]\n"
  "ds_read_b128 v[212:215], v247 offset:63488\n"
  "s_add_u32 m0, s45, 0xc000\n"
  "s_add_u32 s68, s52, 0x78000\n"
  "buffer_load_dwordx4 v240, s[36:39], s68 offen lds\n"
  "s_waitcnt lgkmcnt(3)\n"
  "v_mfma_f32_16x16x32_bf16 v[136:139], v[216:219], v[16:19], v[136:139]\n"
  "s_add_u32 s71, s56, 0xc000\n"
  "buffer_load_dwordx4 v[64:67], v240, s[36:39], s71 offen sc1\n"
  "v_mfma_f32_16x16x32_bf16 v[140:143], v[216:219], v[20:23], v[140:143]\n"
  "ds_read_b128 v[216:219], v248 offset:0\n"
  "s_waitcnt lgkmcnt(3)\n"
  "v_mfma_f32_16x16x32_bf16 v[96:99], v[220:223], v[16:19], v[96:99]\n"
  "s_add_u32 m0, s45, 0xe000\n"
  "s_add_u32 s68, s52, 0x7a000\n"
  "buffer_load_dwordx4 v240, s[36:39], s68 offen lds\n"
  "v_mfma_f32_16x16x32_bf16 v[100:103], v[220:223], v[20:23], v[100:103]\n"
  "ds_read_b128 v[220:223], v248 offset:2048\n"
  "s_waitcnt lgkmcnt(3)\n"
  "v_mfma_f32_16x16x32_bf16 v[112:115], v[208:211], v[16:19], v[112:115]\n"
  "buffer_load_dwordx4 v[68:71], v240, s[36:39], s71 offen offset:1024 sc1\n"
  "v_mfma_f32_16x16x32_bf16 v[116:119], v[208:211], v[20:23], v[116:119]\n"
  "ds_read_b128 v[208:211], v248 offset:4096\n"
  "s_waitcnt lgkmcnt(3)\n"
  "v_mfma_f32_16x16x32_bf16 v[128:131], v[212:215], v[16:19], v[128:131]\n"
  "s_add_u32 m0, s45, 0x10000\n"
  "s_add_u32 s68, s52, 0x7c000\n"
  "buffer_load_dwordx4 v240, s[36:39], s68 offen lds\n"
  "v_mfma_f32_16x16x32_bf16 v[132:135], v[212:215], v[20:23], v[132:135]\n"
  "ds_read_b128 v[212:215], v248 offset:6144\n"
  "s_waitcnt lgkmcnt(3)\n"
  "v_mfma_f32_16x16x32_bf16 v[88:91], v[216:219], v[16:19], v[88:91]\n"
  "buffer_load_dwordx4 v[72:75], v240, s[36:39], s71 offen offset:2048 sc1\n"
  "v_mfma_f32_16x16x32_bf16 v[92:95], v[216:219], v[20:23], v[92:95]\n"
  "ds_read_b128 v[216:219], v248 offset:8192\n"
  "s_waitcnt lgkmcnt(3)\n"
  "v_mfma_f32_16x16x32_bf16 v[104:107], v[220:223], v[16:19], v[104:107]\n"
  "s_add_u32 m0, s45, 0x12000\n"
  "s_add_u32 s68, s52, 0x7e000\n"
  "buffer_load_dwordx4 v240, s[36:39], s68 offen lds\n"
  "v_mfma_f32_16x16x32_bf16 v[108:111], v[220:223], v[20:23], v[108:111]\n"
  "ds_read_b128 v[220:223], v248 offset:10240\n"
  "s_waitcnt lgkmcnt(3)\n"
  "v_mfma_f32_16x16x32_bf16 v[120:123], v[208:211], v[16:19], v[120:123]\n"
  "v_swap_b32 v26, v28\n"
  "v_swap_b32 v27, v29\n"
  "v_mfma_f32_16x16x32_bf16 v[124:127], v[208:211], v[20:23], v[124:127]\n"
  "ds_read_b128 v[208:211], v248 offset:12288\n"
  "buffer_load_dwordx4 v[76:79], v240, s[36:39], s71 offen offset:3072 sc1\n"
  "s_waitcnt lgkmcnt(3)\n"
  "v_mfma_f32_16x16x32_bf16 v[136:139], v[212:215], v[24:27], v[136:139]\n"
  "s_add_u32 m0, s45, 0x14000\n"
  "s_add_u32 s68, s52, 0x80000\n"
  "buffer_load_dwordx4 v240, s[36:39], s68 offen lds\n"
  "v_mfma_f32_16x16x32_bf16 v[140:143], v[212:215], v[28:31], v[140:143]\n"
  "ds_read_b128 v[212:215], v248 offset:14336\n"
  "s_waitcnt lgkmcnt(3)\n"
  "v_mfma_f32_16x16x32_bf16 v[96:99], v[216:219], v[24:27], v[96:99]\n"
  "v_mfma_f32_16x16x32_bf16 v[100:103], v[216:219], v[28:31], v[100:103]\n"
  "ds_read_b128 v[216:219], v246 offset:0\n"
  "s_waitcnt lgkmcnt(3)\n"
  "v_mfma_f32_16x16x32_bf16 v[112:115], v[220:223], v[24:27], v[112:115]\n"
  "s_add_u32 m0, s45, 0x16000\n"
  "s_add_u32 s68, s52, 0x82000\n"
  "buffer_load_dwordx4 v240, s[36:39], s68 offen lds\n"
  "v_mfma_f32_16x16x32_bf16 v[116:119], v[220:223], v[28:31], v[116:119]\n"
  "ds_read_b128 v[220:223], v246 offset:2048\n"
  "s_waitcnt lgkmcnt(3)\n"
  "v_mfma_f32_16x16x32_bf16 v[128:131], v[208:211], v[24:27], v[128:131]\n"
  "v_mfma_f32_16x16x32_bf16 v[132:135], v[208:211], v[28:31], v[132:135]\n"
  "ds_read_b128 v[208:211], v246 offset:4096\n"
  "s_waitcnt lgkmcnt(3)\n"
  "v_mfma_f32_16x16x32_bf16 v[88:91], v[212:215], v[24:27], v[88:91]\n"
  "s_add_u32 m0, s45, 0x18000\n"
  "s_add_u32 s68, s52, 0x84000\n"
  "buffer_load_dwordx4 v240, s[36:39], s68 offen lds\n"
  "v_mfma_f32_16x16x32_bf16 v[92:95], v[212:215], v[28:31], v[92:95]\n"
  "ds_read_b128 v[212:215], v246 offset:6144\n"
  "s_waitcnt lgkmcnt(3)\n"
  "v_mfma_f32_16x16x32_bf16 v[104:107], v[216:219], v[24:27], v[104:107]\n"
  "v_mfma_f32_16x16x32_bf16 v[108:111], v[216:219], v[28:31], v[108:111]\n"
  "ds_read_b128 v[216:219], v246 offset:8192\n"
  "s_waitcnt lgkmcnt(3)\n"
  "v_mfma_f32_16x16x32_bf16 v[120:123], v[220:223], v[24:27], v[120:123]\n"
  "v_swap_b32 v34, v36\n"
  "v_swap_b32 v35, v37\n"
  "v_mfma_f32_16x16x32_bf16 v[124:127], v[220:223], v[28:31], v[124:127]\n"
  "ds_read_b128 v[220:223], v246 offset:10240\n"
  "s_add_u32 m0, s45, 0x1a000\n"
  "s_add_u32 s68, s52, 0x86000\n"
  "buffer_load_dwordx4 v240, s[36:39], s68 offen lds\n"
  "s_waitcnt lgkmcnt(3)\n"
  "v_mfma_f32_16x16x32_bf16 v[136:139], v[208:211], v[32:35], v[136:139]\n"
  "v_mfma_f32_16x16x32_bf16 v[140:143], v[208:211], v[36:39], v[140:143]\n"
  "ds_read_b128 v[208:211], v246 offset:12288\n"
  "s_waitcnt lgkmcnt(3)\n"
  "v_mfma_f32_16x16x32_bf16 v[96:99], v[212:215], v[32:35], v[96:99]\n"
  "v_mfma_f32_16x16x32_bf16 v[100:103], v[212:215], v[36:39], v[100:103]\n"
  "ds_read_b128 v[212:215], v246 offset:14336\n"
  "s_waitcnt lgkmcnt(3)\n"
  "v_mfma_f32_16x16x32_bf16 v[112:115], v[216:219], v[32:35], v[112:115]\n"
  "v_mfma_f32_16x16x32_bf16 v[116:119], v[216:219], v[36:39], v[116:119]\n"
  "ds_read_b128 v[216:219], v246 offset:16384\n"
  "s_waitcnt lgkmcnt(3)\n"
  "v_mfma_f32_16x16x32_bf16 v[128:131], v[220:223], v[32:35], v[128:131]\n"
  "v_mfma_f32_16x16x32_bf16 v[132:135], v[220:223], v[36:39], v[132:135]\n"
  "ds_read_b128 v[220:223], v246 offset:18432\n"
  "s_waitcnt lgkmcnt(3)\n"
  "v_mfma_f32_16x16x32_bf16 v[88:91], v[208:211], v[32:35], v[88:91]\n"
  "v_mfma_f32_16x16x32_bf16 v[92:95], v[208:211], v[36:39], v[92:95]\n"
  "ds_read_b128 v[208:211], v246 offset:20480\n"
  "s_waitcnt lgkmcnt(3)\n"
  "v_mfma_f32_16x16x32_bf16 v[104:107], v[212:215], v[32:35], v[104:107]\n"
  "v_mfma_f32_16x16x32_bf16 v[108:111], v[212:215], v[36:39], v[108:111]\n"
  "ds_read_b128 v[212:215], v246 offset:22528\n"
  "s_waitcnt lgkmcnt(3)\n"
  "v_mfma_f32_16x16x32_bf16 v[120:123], v[216:219], v[32:35], v[120:123]\n"
  "v_swap_b32 v42, v44\n"
  "v_swap_b32 v43, v45\n"
  "v_mfma_f32_16x16x32_bf16 v[124:127], v[216:219], v[36:39], v[124:127]\n"
  "ds_read_b128 v[216:219], v246 offset:24576\n"
  "s_waitcnt lgkmcnt(3)\n"
  "v_mfma_f32_16x16x32_bf16 v[136:139], v[220:223], v[40:43], v[136:139]\n"
  "v_mfma_f32_16x16x32_bf16 v[140:143], v[220:223], v[44:47], v[140:143]\n"
  "ds_read_b128 v[220:223], v246 offset:26624\n"
  "s_waitcnt lgkmcnt(3)\n"
  "v_mfma_f32_16x16x32_bf16 v[96:99], v[208:211], v[40:43], v[96:99]\n"
  "buffer_load_dword v249, v250, s[36:39], s55 offen sc1\n"
  "v_mfma_f32_16x16x32_bf16 v[100:103], v[208:211], v[44:47], v[100:103]\n"
  "ds_read_b128 v[208:211], v246 offset:28672\n"
  "s_waitcnt lgkmcnt(3)\n"
  "v_mfma_f32_16x16x32_bf16 v[112:115], v[212:215], v[40:43], v[112:115]\n"
  "v_mfma_f32_16x16x32_bf16 v[116:119], v[212:215], v[44:47], v[116:119]\n"
  "ds_read_b128 v[212:215], v246 offset:30720\n"
  "s_waitcnt lgkmcnt(3)\n"
  "v_mfma_f32_16x16x32_bf16 v[128:131], v[216:219], v[40:43], v[128:131]\n"
  "v_mfma_f32_16x16x32_bf16 v[132:135], v[216:219], v[44:47], v[132:135]\n"
  "ds_read_b128 v[216:219], v246 offset:32768\n"
  "s_waitcnt lgkmcnt(3)\n"
  "v_mfma_f32_16x16x32_bf16 v[88:91], v[220:223], v[40:43], v[88:91]\n"
  "v_mfma_f32_16x16x32_bf16 v[92:95], v[220:223], v[44:47], v[92:95]\n"
  "ds_read_b128 v[220:223], v246 offset:34816\n"
  "s_waitcnt lgkmcnt(3)\n"
  "v_mfma_f32_16x16x32_bf16 v[104:107], v[208:211], v[40:43], v[104:107]\n"
  "v_mfma_f32_16x16x32_bf16 v[108:111], v[208:211], v[44:47], v[108:111]\n"
  "ds_read_b128 v[208:211], v246 offset:36864\n"
  "s_waitcnt lgkmcnt(3)\n"
  "v_mfma_f32_16x16x32_bf16 v[120:123], v[212:215], v[40:43], v[120:123]\n"
  "v_swap_b32 v50, v52\n"
  "v_swap_b32 v51, v53\n"
  "v_mfma_f32_16x16x32_bf16 v[124:127], v[212:215], v[44:47], v[124:127]\n"
  "ds_read_b128 v[212:215], v246 offset:38912\n"
  "s_waitcnt lgkmcnt(3)\n"
  "v_mfma_f32_16x16x32_bf16 v[136:139], v[216:219], v[48:51], v[136:139]\n"
  "v_mfma_f32_16x16x32_bf16 v[140:143], v[216:219], v[52:55], v[140:143]\n"
  "ds_read_b128 v[216:219], v246 offset:40960\n"
  "s_waitcnt lgkmcnt(3)\n"
  "v_mfma_f32_16x16x32_bf16 v[96:99], v[220:223], v[48:51], v[96:99]\n"
  "v_mfma_f32_16x16x32_bf16 v[100:103], v[220:223], v[52:55], v[100:103]\n"
  "ds_read_b128 v[220:223], v246 offset:43008\n"
  "s_waitcnt vmcnt(1)\n"
  "s_barrier\n"
  "s_waitcnt lgkmcnt(3)\n"
  "v_mfma_f32_16x16x32_bf16 v[112:115], v[208:211], v[48:51], v[112:115]\n"
  "v_mfma_f32_16x16x32_bf16 v[116:119], v[208:211], v[52:55], v[116:119]\n"
  "ds_read_b128 v[208:211], v246 offset:45056\n"
  "s_waitcnt lgkmcnt(3)\n"
  "v_mfma_f32_16x16x32_bf16 v[128:131], v[212:215], v[48:51], v[128:131]\n"
  "s_add_u32 m0, s45, 0x1c000\n"
  "s_add_u32 s68, s52, 0x88000\n"
  "buffer_load_dwordx4 v240, s[36:39], s68 offen lds\n"
  "v_mfma_f32_16x16x32_bf16 v[132:135], v[212:215], v[52:55], v[132:135]\n"
  "ds_read_b128 v[212:215], v246 offset:47104\n"
  "s_waitcnt lgkmcnt(3)\n"
  "v_mfma_f32_16x16x32_bf16 v[88:91], v[216:219], v[48:51], v[88:91]\n"
  "v_mfma_f32_16x16x32_bf16 v[92:95], v[216:219], v[52:55], v[92:95]\n"
  "ds_read_b128 v[216:219], v246 offset:49152\n"
  "s_waitcnt lgkmcnt(3)\n"
  "v_mfma_f32_16x16x32_bf16 v[104:107], v[220:223], v[48:51], v[104:107]\n"
  "s_add_u32 m0, s45, 0x1e000\n"
  "s_add_u32 s68, s52, 0x8a000\n"
  "buffer_load_dwordx4 v240, s[36:39], s68 offen lds\n"
  "v_mfma_f32_16x16x32_bf16 v[108:111], v[220:223], v[52:55], v[108:111]\n"
  "ds_read_b128 v[220:223], v246 offset:51200\n"
  "s_waitcnt lgkmcnt(3)\n"
  "v_mfma_f32_16x16x32_bf16 v[120:123], v[208:211], v[48:51], v[120:123]\n"
  "v_swap_b32 v58, v60\n"
  "v_swap_b32 v59, v61\n"
  "v_mfma_f32_16x16x32_bf16 v[124:127], v[208:211], v[52:55], v[124:127]\n"
  "ds_read_b128 v[208:211], v246 offset:53248\n"
  "s_lshl_b32 s70, s45, 3\n"
  "v_add_u32 v253, s70, v241\n"
  "s_waitcnt lgkmcnt(3)\n"
  "v_mfma_f32_16x16x32_bf16 v[136:139], v[212:215], v[56:59], v[136:139]\n"
  "s_add_u32 m0, s45, 0x20000\n"
  "s_add_u32 s68, s52, 0x8c000\n"
  "buffer_load_dwordx4 v240, s[36:39], s68 offen lds\n"
  "v_mfma_f32_16x16x32_bf16 v[140:143], v[212:215], v[60:63], v[140:143]\n"
  "ds_read_b128 v[212:215], v246 offset:55296\n"
  "s_waitcnt lgkmcnt(3)\n"
  "v_mfma_f32_16x16x32_bf16 v[96:99], v[216:219], v[56:59], v[96:99]\n"
  "v_xor_b32 v252, 0x8000, v253\n"
  "v_mfma_f32_16x16x32_bf16 v[100:103], v[216:219], v[60:63], v[100:103]\n"
  "ds_read_b128 v[216:219], v246 offset:57344\n"
  "s_waitcnt lgkmcnt(3)\n"
  "v_mfma_f32_16x16x32_bf16 v[112:115], v[220:223], v[56:59], v[112:115]\n"
  "s_add_u32 m0, s45, 0x22000\n"
  "s_add_u32 s68, s52, 0x8e000\n"
  "buffer_load_dwordx4 v240, s[36:39], s68 offen lds\n"
  "v_mfma_f32_16x16x32_bf16 v[116:119], v[220:223], v[60:63], v[116:119]\n"
  "ds_read_b128 v[220:223], v246 offset:59392\n"
  "s_waitcnt vmcnt(4)\n"
  "v_cmp_gt_u32 vcc, 9, v249\n"
  "s_cbranch_vccnz .Lk34_fl9\n"
  ".Lk34_fl9_r:\n"
  "s_waitcnt lgkmcnt(3)\n"
  "v_mfma_f32_16x16x32_bf16 v[128:131], v[208:211], v[56:59], v[128:131]\n"
  "s_add_u32 s71, s56, 0xe000\n"
  "buffer_load_dwordx4 v[160:163], v240, s[36:39], s71 offen sc1\n"
  "v_mfma_f32_16x16x32_bf16 v[132:135], v[208:211], v[60:63], v[132:135]\n"
  "ds_read_b128 v[208:211], v246 offset:61440\n"
  "s_waitcnt lgkmcnt(3)\n"
  "v_mfma_f32_16x16x32_bf16 v[88:91], v[212:215], v[56:59], v[88:91]\n"
  "s_add_u32 m0, s45, 0\n"
  "s_add_u32 s68, s52, 0x90000\n"
  "buffer_load_dwordx4 v240, s[36:39], s68 offen lds\n"
  "v_mfma_f32_16x16x32_bf16 v[92:95], v[212:215], v[60:63], v[92:95]\n"
  "ds_read_b128 v[212:215], v246 offset:63488\n"
  "s_waitcnt lgkmcnt(3)\n"
  "v_mfma_f32_16x16x32_bf16 v[104:107], v[216:219], v[56:59], v[104:107]\n"
  "buffer_load_dwordx4 v[164:167], v240, s[36:39], s71 offen offset:1024 sc1\n"
  "v_mfma_f32_16x16x32_bf16 v[108:111], v[216:219], v[60:63], v[108:111]\n"
  "ds_read_b128 v[216:219], v247 offset:0\n"
  "s_waitcnt lgkmcnt(3)\n"
  "v_mfma_f32_16x16x32_bf16 v[120:123], v[220:223], v[56:59], v[120:123]\n"
  "v_swap_b32 v66, v68\n"
  "v_swap_b32 v67, v69\n"
  "v_mfma_f32_16x16x32_bf16 v[124:127], v[220:223], v[60:63], v[124:127]\n"
  "ds_read_b128 v[220:223], v247 offset:2048\n"
  "s_add_u32 m0, s45, 0x2000\n"
  "s_add_u32 s68, s52, 0x92000\n"
  "buffer_load_dwordx4 v240, s[36:39], s68 offen lds\n"
  "s_waitcnt lgkmcnt(3)\n"
  "v_mfma_f32_16x16x32_bf16 v[136:139], v[208:211], v[64:67], v[136:139]\n"
  "buffer_load_dwordx4 v[168:171], v240, s[36:39], s71 offen offset:2048 sc1\n"
  "v_mfma_f32_16x16x32_bf16 v[140:143], v[208:211], v[68:71], v[140:143]\n"
  "ds_read_b128 v[208:211], v247 offset:4096\n"
  "s_waitcnt lgkmcnt(3)\n"
  "v_mfma_f32_16x16x32_bf16 v[96:99], v[212:215], v[64:67], v[96:99]\n"
  "s_add_u32 m0, s45, 0x4000\n"
  "s_add_u32 s68, s52, 0x94000\n"
  "buffer_load_dwordx4 v240, s[36:39], s68 offen lds\n"
  "v_mfma_f32_16x16x32_bf16 v[100:103], v[212:215], v[68:71], v[100:103]\n"
  "ds_read_b128 v[212:215], v247 offset:6144\n"
  "s_waitcnt lgkmcnt(3)\n"
  "v_mfma_f32_16x16x32_bf16 v[112:115], v[216:219], v[64:67], v[112:115]\n"
  "buffer_load_dwordx4 v[172:175], v240, s[36:39], s71 offen offset:3072 sc1\n"
  "v_mfma_f32_16x16x32_bf16 v[116:119], v[216:219], v[68:71], v[116:119]\n"
  "ds_read_b128 v[216:219], v247 offset:8192\n"
  "s_waitcnt lgkmcnt(3)\n"
  "v_mfma_f32_16x16x32_bf16 v[128:131], v[220:223], v[64:67], v[128:131]\n"
  "s_add_u32 m0, s45, 0x6000\n"
  "s_add_u32 s68, s52, 0x96000\n"
  "buffer_load_dwordx4 v240, s[36:39], s68 offen lds\n"
  "v_mfma_f32_16x16x32_bf16 v[132:135], v[220:223], v[68:71], v[132:135]\n"
  "ds_read_b128 v[220:223], v247 offset:10240\n"
  "s_waitcnt lgkmcnt(3)\n"
  "v_mfma_f32_16x16x32_bf16 v[88:91], v[208:211], v[64:67], v[88:91]\n"
  "s_add_u32 s71, s56, 0x10000\n"
  "buffer_load_dwordx4 v[144:147], v240, s[36:39], s71 offen sc1\n"
  "v_mfma_f32_16x16x32_bf16 v[92:95], v[208:211], v[68:71], v[92:95]\n"
  "ds_read_b128 v[208:211], v247 offset:12288\n"
  "s_waitcnt lgkmcnt(3)\n"
  "v_mfma_f32_16x16x32_bf16 v[104:107], v[212:215], v[64:67], v[104:107]\n"
  "buffer_load_dwordx4 v[148:151], v240, s[36:39], s71 offen offset:1024 sc1\n"
  "v_mfma_f32_16x16x32_bf16 v[108:111], v[212:215], v[68:71], v[108:111]\n"
  "ds_read_b128 v[212:215], v247 offset:14336\n"
  "s_waitcnt lgkmcnt(3)\n"
  "v_mfma_f32_16x16x32_bf16 v[120:123], v[216:219], v[64:67], v[120:123]\n"
  "v_swap_b32 v74, v76\n"
  "v_swap_b32 v75, v77\n"
  "v_mfma_f32_16x16x32_bf16 v[124:127], v[216:219], v[68:71], v[124:127]\n"
  "ds_read_b128 v[216:219], v247 offset:16384\n"
  "buffer_load_dwordx4 v[152:155], v240, s[36:39], s71 offen offset:2048 sc1\n"
  "s_waitcnt lgkmcnt(3)\n"
  "v_mfma_f32_16x16x32_bf16 v[136:139], v[220:223], v[72:75], v[136:139]\n"
  "buffer_load_dwordx4 v[156:159], v240, s[36:39], s71 offen offset:3072 sc1\n"
  "v_mfma_f32_16x16x32_bf16 v[140:143], v[220:223], v[76:79], v[140:143]\n"
  "ds_read_b128 v[220:223], v247 offset:18432\n"
  "s_waitcnt lgkmcnt(3)\n"
  "v_mfma_f32_16x16x32_bf16 v[96:99], v[208:211], v[72:75], v[96:99]\n"
  "v_mfma_f32_16x16x32_bf16 v[100:103], v[208:211], v[76:79], v[100:103]\n"
  "ds_read_b128 v[208:211], v247 offset:20480\n"
  "s_waitcnt lgkmcnt(3)\n"
  "v_mfma_f32_16x16x32_bf16 v[112:115], v[212:215], v[72:75], v[112:115]\n"
  "v_mfma_f32_16x16x32_bf16 v[116:119], v[212:215], v[76:79], v[116:119]\n"
  "ds_read_b128 v[212:215], v247 offset:22528\n"
  "s_waitcnt lgkmcnt(3)\n"
  "v_mfma_f32_16x16x32_bf16 v[128:131], v[216:219], v[72:75], v[128:131]\n"
  "v_mfma_f32_16x16x32_bf16 v[132:135], v[216:219], v[76:79], v[132:135]\n"
  "ds_read_b128 v[216:219], v247 offset:24576\n"
  "s_waitcnt lgkmcnt(3)\n"
  "v_mfma_f32_16x16x32_bf16 v[88:91], v[220:223], v[72:75], v[88:91]\n"
  "v_mfma_f32_16x16x32_bf16 v[92:95], v[220:223], v[76:79], v[92:95]\n"
  "ds_read_b128 v[220:223], v247 offset:26624\n"
  "s_waitcnt lgkmcnt(3)\n"
  "v_mfma_f32_16x16x32_bf16 v[104:107], v[208:211], v[72:75], v[104:107]\n"
  "v_mfma_f32_16x16x32_bf16 v[108:111], v[208:211], v[76:79], v[108:111]\n"
  "ds_read_b128 v[208:211], v247 offset:28672\n"
  "s_waitcnt lgkmcnt(3)\n"
  "v_mfma_f32_16x16x32_bf16 v[120:123], v[212:215], v[72:75], v[120:123]\n"
  "s_waitcnt vmcnt(9)\n"
  "v_swap_b32 v162, v164\n"
  "v_swap_b32 v163, v165\n"
  "v_mfma_f32_16x16x32_bf16 v[124:127], v[212:215], v[76:79], v[124:127]\n"
  "ds_read_b128 v[212:215], v247 offset:30720\n"
  "s_waitcnt lgkmcnt(3)\n"
  "v_mfma_f32_16x16x32_bf16 v[136:139], v[216:219], v[160:163], v[136:139]\n"
  "v_mfma_f32_16x16x32_bf16 v[140:143], v[216:219], v[164:167], v[140:143]\n"
  "ds_read_b128 v[216:219], v247 offset:32768\n"
  "s_waitcnt lgkmcnt(3)\n"
  "v_mfma_f32_16x16x32_bf16 v[96:99], v[220:223], v[160:163], v[96:99]\n"
  "v_mfma_f32_16x16x32_bf16 v[100:103], v[220:223], v[164:167], v[100:103]\n"
  "ds_read_b128 v[220:223], v247 offset:34816\n"
  "s_waitcnt lgkmcnt(3)\n"
  "v_mfma_f32_16x16x32_bf16 v[112:115], v[208:211], v[160:163], v[112:115]\n"
  "s_and_b32 s73, s54, 0xffffff9f\n"
  "v_lshlrev_b32 v250, 3, v250\n"
  "v_mfma_f32_16x16x32_bf16 v[116:119], v[208:211], v[164:167], v[116:119]\n"
  "ds_read_b128 v[208:211], v247 offset:36864\n"
  "s_waitcnt lgkmcnt(3)\n"
  "v_mfma_f32_16x16x32_bf16 v[128:131], v[212:215], v[160:163], v[128:131]\n"
  "v_mfma_f32_16x16x32_bf16 v[132:135], v[212:215], v[164:167], v[132:135]\n"
  "ds_read_b128 v[212:215], v247 offset:38912\n"
  "s_waitcnt lgkmcnt(3)\n"
  "v_mfma_f32_16x16x32_bf16 v[88:91], v[216:219], v[160:163], v[88:91]\n"
  "buffer_load_dword v249, v250, s[36:39], s73 offen sc1\n"
  "v_mfma_f32_16x16x32_bf16 v[92:95], v[216:219], v[164:167], v[92:95]\n"
  "ds_read_b128 v[216:219], v247 offset:40960\n"
  "s_waitcnt lgkmcnt(3)\n"
  "v_mfma_f32_16x16x32_bf16 v[104:107], v[220:223], v[160:163], v[104:107]\n"
  "v_mfma_f32_16x16x32_bf16 v[108:111], v[220:223], v[164:167], v[108:111]\n"
  "ds_read_b128 v[220:223], v247 offset:43008\n"
  "s_barrier\n"
  "s_waitcnt lgkmcnt(3)\n"
  "v_mfma_f32_16x16x32_bf16 v[120:123], v[208:211], v[160:163], v[120:123]\n"
  "s_waitcnt vmcnt(6)\n"
  "v_swap_b32 v170, v172\n"
  "v_swap_b32 v171, v173\n"
  "v_mfma_f32_16x16x32_bf16 v[124:127], v[208:211], v[164:167], v[124:127]\n"
  "ds_read_b128 v[208:211], v247 offset:45056\n"
  "s_waitcnt lgkmcnt(3)\n"
  "v_mfma_f32_16x16x32_bf16 v[136:139], v[212:215], v[168:171], v[136:139]\n"
  "s_add_u32 m0, s45, 0x8000\n"
  "s_add_u32 s68, s52, 0x98000\n"
  "buffer_load_dwordx4 v240, s[36:39], s68 offen lds\n"
  "v_mfma_f32_16x16x32_bf16 v[140:143], v[212:215], v[172:175], v[140:143]\n"
  "ds_read_b128 v[212:215], v247 offset:47104\n"
  "s_waitcnt lgkmcnt(3)\n"
  "v_mfma_f32_16x16x32_bf16 v[96:99], v[216:219], v[168:171], v[96:99]\n"
  "v_mfma_f32_16x16x32_bf16 v[100:103], v[216:219], v[172:175], v[100:103]\n"
  "ds_read_b128 v[216:219], v247 offset:49152\n"
  "s_waitcnt lgkmcnt(3)\n"
  "v_mfma_f32_16x16x32_bf16 v[112:115], v[220:223], v[168:171], v[112:115]\n"
  "s_add_u32 m0, s45, 0xa000\n"
  "s_add_u32 s68, s52, 0x9a000\n"
  "buffer_load_dwordx4 v240, s[36:39], s68 offen lds\n"
  "v_mfma_f32_16x16x32_bf16 v[116:119], v[220:223], v[172:175], v[116:119]\n"
  "ds_read_b128 v[220:223], v247 offset:51200\n"
  "s_waitcnt lgkmcnt(3)\n"
  "v_mfma_f32_16x16x32_bf16 v[128:131], v[208:211], v[168:171], v[128:131]\n"
  "v_mfma_f32_16x16x32_bf16 v[132:135], v[208:211], v[172:175], v[132:135]\n"
  "ds_read_b128 v[208:211], v247 offset:53248\n"
  "s_waitcnt lgkmcnt(3)\n"
  "v_mfma_f32_16x16x32_bf16 v[88:91], v[212:215], v[168:171], v[88:91]\n"
  "s_add_u32 m0, s45, 0xc000\n"
  "s_add_u32 s68, s52, 0x9c000\n"
  "buffer_load_dwordx4 v240, s[36:39], s68 offen lds\n"
  "v_mfma_f32_16x16x32_bf16 v[92:95], v[212:215], v[172:175], v[92:95]\n"
  "ds_read_b128 v[212:215], v247 offset:55296\n"
  "s_waitcnt lgkmcnt(3)\n"
  "v_mfma_f32_16x16x32_bf16 v[104:107], v[216:219], v[168:171], v[104:107]\n"
  "v_mfma_f32_16x16x32_bf16 v[108:111], v[216:219], v[172:175], v[108:111]\n"
  "ds_read_b128 v[216:219], v247 offset:57344\n"
  "s_waitcnt lgkmcnt(3)\n"
  "v_mfma_f32_16x16x32_bf16 v[120:123], v[220:223], v[168:171], v[120:123]\n"
  "s_waitcnt vmcnt(6)\n"
  "v_swap_b32 v146, v148\n"
  "v_swap_b32 v147, v149\n"
  "v_mfma_f32_16x16x32_bf16 v[124:127], v[220:223], v[172:175], v[124:127]\n"
  "ds_read_b128 v[220:223], v247 offset:59392\n"
  "s_add_u32 m0, s45, 0xe000\n"
  "s_add_u32 s68, s52, 0x9e000\n"
  "buffer_load_dwordx4 v240, s[36:39], s68 offen lds\n"
  "s_waitcnt lgkmcnt(3)\n"
  "v_mfma_f32_16x16x32_bf16 v[136:139], v[208:211], v[144:147], v[136:139]\n"
  "v_mfma_f32_16x16x32_bf16 v[140:143], v[208:211], v[148:151], v[140:143]\n"
  "ds_read_b128 v[208:211], v247 offset:61440\n"
  "s_waitcnt lgkmcnt(3)\n"
  "v_mfma_f32_16x16x32_bf16 v[96:99], v[212:215], v[144:147], v[96:99]\n"
  "s_add_u32 m0, s45, 0x10000\n"
  "s_add_u32 s68, s52, 0xa0000\n"
  "buffer_load_dwordx4 v240, s[36:39], s68 offen lds\n"
  "v_mfma_f32_16x16x32_bf16 v[100:103], v[212:215], v[148:151], v[100:103]\n"
  "ds_read_b128 v[212:215], v247 offset:63488\n"
  "s_waitcnt lgkmcnt(3)\n"
  "v_mfma_f32_16x16x32_bf16 v[112:115], v[216:219], v[144:147], v[112:115]\n"
  "v_mfma_f32_16x16x32_bf16 v[116:119], v[216:219], v[148:151], v[116:119]\n"
  "ds_read_b128 v[216:219], v248 offset:0\n"
  "s_waitcnt lgkmcnt(3)\n"
  "v_mfma_f32_16x16x32_bf16 v[128:131], v[220:223], v[144:147], v[128:131]\n"
  "v_mfma_f32_16x16x32_bf16 v[132:135], v[220:223], v[148:151], v[132:135]\n"
  "ds_read_b128 v[220:223], v248 offset:2048\n"
  "s_waitcnt lgkmcnt(3)\n"
  "v_mfma_f32_16x16x32_bf16 v[88:91], v[208:211], v[144:147], v[88:91]\n"
  "v_mfma_f32_16x16x32_bf16 v[92:95], v[208:211], v[148:151], v[92:95]\n"
  "ds_read_b128 v[208:211], v248 offset:4096\n"
  "s_waitcnt lgkmcnt(3)\n"
  "v_mfma_f32_16x16x32_bf16 v[104:107], v[212:215], v[144:147], v[104:107]\n"
  "v_mfma_f32_16x16x32_bf16 v[108:111], v[212:215], v[148:151], v[108:111]\n"
  "ds_read_b128 v[212:215], v248 offset:6144\n"
  "s_waitcnt lgkmcnt(3)\n"
  "v_mfma_f32_16x16x32_bf16 v[120:123], v[216:219], v[144:147], v[120:123]\n"
  "s_waitcnt vmcnt(6)\n"
  "v_swap_b32 v154, v156\n"
  "v_swap_b32 v155, v157\n"
  "v_mfma_f32_16x16x32_bf16 v[124:127], v[216:219], v[148:151], v[124:127]\n"
  "ds_read_b128 v[216:219], v248 offset:8192\n"
  "s_waitcnt lgkmcnt(3)\n"
  "v_mfma_f32_16x16x32_bf16 v[136:139], v[220:223], v[152:155], v[136:139]\n"
  "v_mfma_f32_16x16x32_bf16 v[140:143], v[220:223], v[156:159], v[140:143]\n"
  "ds_read_b128 v[220:223], v248 offset:10240\n"
  "s_waitcnt lgkmcnt(3)\n"
  "v_mfma_f32_16x16x32_bf16 v[96:99], v[208:211], v[152:155], v[96:99]\n"
  "s_waitcnt vmcnt(5)\n"
  "v_cmp_gt_u32 vcc, 5, v249\n"
  "s_cbranch_vccnz .Lk34_pf\n"
  ".Lk34_pf_r:\n"
  "v_mfma_f32_16x16x32_bf16 v[100:103], v[208:211], v[156:159], v[100:103]\n"
  "ds_read_b128 v[208:211], v248 offset:12288\n"
  "s_waitcnt lgkmcnt(3)\n"
  "v_mfma_f32_16x16x32_bf16 v[112:115], v[212:215], v[152:155], v[112:115]\n"
  "v_mfma_f32_16x16x32_bf16 v[116:119], v[212:215], v[156:159], v[116:119]\n"
  "ds_read_b128 v[212:215], v248 offset:14336\n"
  "s_barrier\n"
  "s_waitcnt lgkmcnt(3)\n"
  "v_mfma_f32_16x16x32_bf16 v[128:131], v[216:219], v[152:155], v[128:131]\n"
  "ds_write_b128 v252, v[136:139] offset:8192\n"
  "v_mfma_f32_16x16x32_bf16 v[132:135], v[216:219], v[156:159], v[132:135]\n"
  "ds_read_b128 v[216:219], v246 offset:0\n"
  "ds_write_b128 v252, v[140:143] offset:9216\n"
  "s_waitcnt lgkmcnt(5)\n"
  "v_mfma_f32_16x16x32_bf16 v[88:91], v[220:223], v[152:155], v[88:91]\n"
  "ds_write_b128 v252, v[96:99] offset:10240\n"
  "v_mfma_f32_16x16x32_bf16 v[92:95], v[220:223], v[156:159], v[92:95]\n"
  "ds_read_b128 v[220:223], v246 offset:2048\n"
  "ds_write_b128 v252, v[100:103] offset:11264\n"
  "s_waitcnt lgkmcnt(7)\n"
  "v_mfma_f32_16x16x32_bf16 v[104:107], v[208:211], v[152:155], v[104:107]\n"
  "ds_write_b128 v252, v[112:115] offset:12288\n"
  "v_mfma_f32_16x16x32_bf16 v[108:111], v[208:211], v[156:159], v[108:111]\n"
  "ds_read_b128 v[208:211], v246 offset:4096\n"
  "ds_write_b128 v252, v[116:119] offset:13312\n"
  "s_waitcnt lgkmcnt(9)\n"
  "v_mfma_f32_16x16x32_bf16 v[120:123], v[212:215], v[152:155], v[120:123]\n"
  "ds_write_b128 v252, v[128:131] offset:14336\n"
  "v_mfma_f32_16x16x32_bf16 v[124:127], v[212:215], v[156:159], v[124:127]\n"
  "ds_read_b128 v[212:215], v246 offset:6144\n"
  "ds_write_b128 v252, v[132:135] offset:15360\n"
  "s_waitcnt lgkmcnt(10)\n"
  "v_mfma_f32_16x16x32_bf16 v[228:231], v[216:219], v[176:179], 0\n"
  "v_mfma_f32_16x16x32_bf16 v[232:235], v[216:219], v[180:183], 0\n"
  "ds_read_b128 v[216:219], v246 offset:8192\n"
  "s_waitcnt vmcnt(2) lgkmcnt(1)\n"
  "s_barrier\n"
  "v_mfma_f32_16x16x32_bf16 v[228:231], v[220:223], v[184:187], v[228:231]\n"
  "ds_read_b128 v[224:227], v253 offset:8192\n"
  "ds_read_b128 v[236:239], v253 offset:9216\n"
  "v_mfma_f32_16x16x32_bf16 v[232:235], v[220:223], v[188:191], v[232:235]\n"
  "ds_read_b128 v[220:223], v246 offset:10240\n"
  "v_mfma_f32_16x16x32_bf16 v[228:231], v[208:211], v[192:195], v[228:231]\n"
  "ds_read_b128 v[96:99], v253 offset:10240\n"
  "ds_read_b128 v[100:103], v253 offset:11264\n"
  "v_mfma_f32_16x16x32_bf16 v[232:235], v[208:211], v[196:199], v[232:235]\n"
  "ds_read_b128 v[208:211], v246 offset:12288\n"
  "v_mfma_f32_16x16x32_bf16 v[228:231], v[212:215], v[200:203], v[228:231]\n"
  "ds_read_b128 v[112:115], v253 offset:12288\n"
  "ds_read_b128 v[116:119], v253 offset:13312\n"
  "v_mfma_f32_16x16x32_bf16 v[232:235], v[212:215], v[204:207], v[232:235]\n"
  "ds_read_b128 v[212:215], v246 offset:14336\n"
  "s_waitcnt lgkmcnt(9)\n"
  "v_mfma_f32_16x16x32_bf16 v[228:231], v[216:219], v[0:3], v[228:231]\n"
  "ds_read_b128 v[128:131], v253 offset:14336\n"
  "ds_read_b128 v[132:135], v253 offset:15360\n"
  "s_waitcnt lgkmcnt(10)\n"
  "v_add_f32 v136, v136, v224\n"
  "v_add_f32 v137, v137, v225\n"
  "v_mfma_f32_16x16x32_bf16 v[232:235], v[216:219], v[4:7], v[232:235]\n"
  "ds_read_b128 v[216:219], v246 offset:16384\n"
  "v_add_f32 v138, v138, v226\n"
  "v_add_f32 v139, v139, v227\n"
  "s_waitcnt lgkmcnt(10)\n"
  "v_add_f32 v140, v140, v236\n"
  "v_add_f32 v141, v141, v237\n"
  "s_waitcnt lgkmcnt(9)\n"
  "v_mfma_f32_16x16x32_bf16 v[228:231], v[220:223], v[8:11], v[228:231]\n"
  "v_add_f32 v142, v142, v238\n"
  "v_add_f32 v143, v143, v239\n"
  "v_cvt_pk_bf16_f32 v224, v136, v137\n"
  "v_mfma_f32_16x16x32_bf16 v[232:235], v[220:223], v[12:15], v[232:235]\n"
  "ds_read_b128 v[220:223], v246 offset:18432\n"
  "v_cvt_pk_bf16_f32 v225, v138, v139\n"
  "v_cvt_pk_bf16_f32 v226, v140, v141\n"
  "v_cvt_pk_bf16_f32 v227, v142, v143\n"
  "s_waitcnt lgkmcnt(7)\n"
  "v_mfma_f32_16x16x32_bf16 v[228:231], v[208:211], v[16:19], v[228:231]\n"
  "v_min_u32 v224, -2, v224\n"
  "v_min_u32 v225, -2, v225\n"
  "v_min_u32 v226, -2, v226\n"
  "v_mfma_f32_16x16x32_bf16 v[232:235], v[208:211], v[20:23], v[232:235]\n"
  "ds_read_b128 v[208:211], v246 offset:20480\n"
  "v_min_u32 v227, -2, v227\n"
  "s_cmp_lg_u32 s62, 1\n"
  "s_cbranch_scc1 .Lk34_ns8\n"
  "buffer_store_dwordx4 v[224:227], v240, s[36:39], s60 offen sc1\n"
  ".Lk34_ns8:\n"
  "v_add_f32 v88, v88, v96\n"
  "v_add_f32 v89, v89, v97\n"
  "s_waitcnt lgkmcnt(5)\n"
  "v_mfma_f32_16x16x32_bf16 v[228:231], v[212:215], v[24:27], v[228:231]\n"
  "v_add_f32 v90, v90, v98\n"
  "v_add_f32 v91, v91, v99\n"
  "v_add_f32 v92, v92, v100\n"
  "v_mfma_f32_16x16x32_bf16 v[232:235], v[212:215], v[28:31], v[232:235]\n"
  "ds_read_b128 v[212:215], v246 offset:22528\n"
  "v_add_f32 v93, v93, v101\n"
  "v_add_f32 v94, v94, v102\n"
  "v_add_f32 v95, v95, v103\n"
  "s_waitcnt lgkmcnt(3)\n"
  "v_mfma_f32_16x16x32_bf16 v[228:231], v[216:219], v[32:35], v[228:231]\n"
  "v_cvt_pk_bf16_f32 v96, v88, v89\n"
  "v_cvt_pk_bf16_f32 v97, v90, v91\n"
  "v_cvt_pk_bf16_f32 v98, v92, v93\n"
  "v_mfma_f32_16x16x32_bf16 v[232:235], v[216:219], v[36:39], v[232:235]\n"
  "ds_read_b128 v[216:219], v246 offset:24576\n"
  "v_cvt_pk_bf16_f32 v99, v94, v95\n"
  "v_min_u32 v96, -2, v96\n"
  "v_min_u32 v97, -2, v97\n"
  "s_waitcnt lgkmcnt(3)\n"
  "v_mfma_f32_16x16x32_bf16 v[228:231], v[220:223], v[40:43], v[228:231]\n"
  "v_min_u32 v98, -2, v98\n"
  "v_min_u32 v99, -2, v99\n"
  "buffer_store_dwordx4 v[96:99], v240, s[36:39], s57 offen sc1\n"
  "v_add_f32 v104, v104, v112\n"
  "v_mfma_f32_16x16x32_bf16 v[232:235], v[220:223], v[44:47], v[232:235]\n"
  "ds_read_b128 v[220:223], v246 offset:26624\n"
  "v_add_f32 v105, v105, v113\n"
  "v_add_f32 v106, v106, v114\n"
  "v_add_f32 v107, v107, v115\n"
  "s_waitcnt lgkmcnt(3)\n"
  "v_mfma_f32_16x16x32_bf16 v[228:231], v[208:211], v[48:51], v[228:231]\n"
  "v_add_f32 v108, v108, v116\n"
  "v_add_f32 v109, v109, v117\n"
  "v_add_f32 v110, v110, v118\n"
  "v_mfma_f32_16x16x32_bf16 v[232:235], v[208:211], v[52:55], v[232:235]\n"
  "ds_read_b128 v[208:211], v246 offset:28672\n"
  "v_add_f32 v111, v111, v119\n"
  "v_cvt_pk_bf16_f32 v112, v104, v105\n"
  "v_cvt_pk_bf16_f32 v113, v106, v107\n"
  "s_waitcnt lgkmcnt(3)\n"
  "v_mfma_f32_16x16x32_bf16 v[228:231], v[212:215], v[56:59], v[228:231]\n"
  "v_cvt_pk_bf16_f32 v114, v108, v109\n"
  "v_cvt_pk_bf16_f32 v115, v110, v111\n"
  "v_min_u32 v112, -2, v112\n"
  "v_mfma_f32_16x16x32_bf16 v[232:235], v[212:215], v[60:63], v[232:235]\n"
  "ds_read_b128 v[212:215], v246 offset:30720\n"
  "v_min_u32 v113, -2, v113\n"
  "v_min_u32 v114, -2, v114\n"
  "v_min_u32 v115, -2, v115\n"
  "buffer_store_dwordx4 v[112:115], v240, s[36:39], s58 offen sc1\n"
  "s_waitcnt lgkmcnt(3)\n"
  "v_mfma_f32_16x16x32_bf16 v[228:231], v[216:219], v[64:67], v[228:231]\n"
  "v_add_f32 v120, v120, v128\n"
  "v_add_f32 v121, v121, v129\n"
  "v_add_f32 v122, v122, v130\n"
  "v_mfma_f32_16x16x32_bf16 v[232:235], v[216:219], v[68:71], v[232:235]\n"
  "ds_read_b128 v[216:219], v246 offset:32768\n"
  "v_add_f32 v123, v123, v131\n"
  "v_add_f32 v124, v124, v132\n"
  "v_add_f32 v125, v125, v133\n"
  "s_waitcnt lgkmcnt(3)\n"
  "v_mfma_f32_16x16x32_bf16 v[228:231], v[220:223], v[72:75], v[228:231]\n"
  "v_add_f32 v126, v126, v134\n"
  "v_add_f32 v127, v127, v135\n"
  "v_cvt_pk_bf16_f32 v128, v120, v121\n"
  "v_mfma_f32_16x16x32_bf16 v[232:235], v[220:223], v[76:79], v[232:235]\n"
  "ds_read_b128 v[220:223], v246 offset:34816\n"
  "v_cvt_pk_bf16_f32 v129, v122, v123\n"
  "v_cvt_pk_bf16_f32 v130, v124, v125\n"
  "v_cvt_pk_bf16_f32 v131, v126, v127\n"
  "s_waitcnt lgkmcnt(3)\n"
  "v_mfma_f32_16x16x32_bf16 v[228:231], v[208:211], v[160:163], v[228:231]\n"
  "v_min_u32 v128, -2, v128\n"
  "v_min_u32 v129, -2, v129\n"
  "v_mfma_f32_16x16x32_bf16 v[232:235], v[208:211], v[164:167], v[232:235]\n"
  "ds_read_b128 v[208:211], v246 offset:36864\n"
  "v_min_u32 v130, -2, v130\n"
  "v_min_u32 v131, -2, v131\n"
  "buffer_store_dwordx4 v[128:131], v240, s[36:39], s59 offen sc1\n"
  "s_waitcnt lgkmcnt(3)\n"
  "v_mfma_f32_16x16x32_bf16 v[228:231], v[212:215], v[168:171], v[228:231]\n"
  "v_mfma_f32_16x16x32_bf16 v[232:235], v[212:215], v[172:175], v[232:235]\n"
  "ds_read_b128 v[212:215], v246 offset:38912\n"
  "s_waitcnt lgkmcnt(3)\n"
  "v_mfma_f32_16x16x32_bf16 v[228:231], v[216:219], v[144:147], v[228:231]\n"
  "s_lshl_b32 s70, s45, 1\n"
  "v_add_u32 v253, s70, v242\n"
  "v_mfma_f32_16x16x32_bf16 v[232:235], v[216:219], v[148:151], v[232:235]\n"
  "ds_read_b128 v[216:219], v246 offset:40960\n"
  "s_waitcnt lgkmcnt(3)\n"
  "v_mfma_f32_16x16x32_bf16 v[228:231], v[220:223], v[152:155], v[228:231]\n"
  "v_xor_b32 v252, 0x2000, v253\n"
  "v_mfma_f32_16x16x32_bf16 v[232:235], v[220:223], v[156:159], v[232:235]\n"
  "ds_read_b128 v[220:223], v246 offset:43008\n"
  "s_waitcnt lgkmcnt(3)\n"
  "v_mfma_f32_16x16x32_bf16 v[80:83], v[208:211], v[176:179], 0\n"
  "v_mfma_f32_16x16x32_bf16 v[84:87], v[208:211], v[180:183], 0\n"
  "ds_read_b128 v[208:211], v246 offset:45056\n"
  "s_waitcnt lgkmcnt(3)\n"
  "v_mfma_f32_16x16x32_bf16 v[80:83], v[212:215], v[184:187], v[80:83]\n"
  "v_mfma_f32_16x16x32_bf16 v[84:87], v[212:215], v[188:191], v[84:87]\n"
  "ds_read_b128 v[212:215], v246 offset:47104\n"
  "s_waitcnt lgkmcnt(3)\n"
  "v_mfma_f32_16x16x32_bf16 v[80:83], v[216:219], v[192:195], v[80:83]\n"
  "ds_write_b128 v252, v[228:231] offset:8192\n"
  "v_mfma_f32_16x16x32_bf16 v[84:87], v[216:219], v[196:199], v[84:87]\n"
  "ds_read_b128 v[216:219], v246 offset:49152\n"
  "s_waitcnt lgkmcnt(4)\n"
  "v_mfma_f32_16x16x32_bf16 v[80:83], v[220:223], v[200:203], v[80:83]\n"
  "ds_write_b128 v252, v[232:235] offset:9216\n"
  "v_mfma_f32_16x16x32_bf16 v[84:87], v[220:223], v[204:207], v[84:87]\n"
  "ds_read_b128 v[220:223], v246 offset:51200\n"
  "s_waitcnt lgkmcnt(5)\n"
  "v_mfma_f32_16x16x32_bf16 v[80:83], v[208:211], v[0:3], v[80:83]\n"
  "v_lshrrev_b32 v244, 8, v240\n"
  "v_lshl_or_b32 v243, v244, 14, v245\n"
  "v_mfma_f32_16x16x32_bf16 v[84:87], v[208:211], v[4:7], v[84:87]\n"
  "ds_read_b128 v[208:211], v246 offset:53248\n"
  "v_mul_u32_u24 v245, 0x90, v245\n"
  "s_waitcnt vmcnt(3) lgkmcnt(2)\n"
  "s_barrier\n"
  "v_mfma_f32_16x16x32_bf16 v[80:83], v[212:215], v[8:11], v[80:83]\n"
  "ds_read_b128 v[228:231], v253 offset:8192\n"
  "v_lshl_add_u32 v245, v244, 3, v245\n"
  "v_mfma_f32_16x16x32_bf16 v[84:87], v[212:215], v[12:15], v[84:87]\n"
  "ds_read_b128 v[212:215], v246 offset:55296\n"
  "v_lshlrev_b32 v244, 4, v244\n"
  "v_mfma_f32_16x16x32_bf16 v[80:83], v[216:219], v[16:19], v[80:83]\n"
  "ds_read_b128 v[232:235], v253 offset:9216\n"
  "v_mfma_f32_16x16x32_bf16 v[84:87], v[216:219], v[20:23], v[84:87]\n"
  "ds_read_b128 v[216:219], v246 offset:57344\n"
  "buffer_load_dwordx4 v[88:91], v243, s[36:39], s63 offen\n"
  "s_waitcnt lgkmcnt(5)\n"
  "v_mfma_f32_16x16x32_bf16 v[80:83], v[220:223], v[24:27], v[80:83]\n"
  "buffer_load_dwordx4 v[92:95], v244, s[36:39], s64 offen\n"
  "v_mfma_f32_16x16x32_bf16 v[84:87], v[220:223], v[28:31], v[84:87]\n"
  "ds_read_b128 v[220:223], v246 offset:59392\n"
  "s_waitcnt lgkmcnt(5)\n"
  "v_mfma_f32_16x16x32_bf16 v[80:83], v[208:211], v[32:35], v[80:83]\n"
  "v_mfma_f32_16x16x32_bf16 v[84:87], v[208:211], v[36:39], v[84:87]\n"
  "ds_read_b128 v[208:211], v246 offset:61440\n"
  "s_waitcnt lgkmcnt(4)\n"
  "v_mfma_f32_16x16x32_bf16 v[80:83], v[212:215], v[40:43], v[80:83]\n"
  "v_mfma_f32_16x16x32_bf16 v[84:87], v[212:215], v[44:47], v[84:87]\n"
  "ds_read_b128 v[212:215], v246 offset:63488\n"
  "s_waitcnt lgkmcnt(3)\n"
  "v_mfma_f32_16x16x32_bf16 v[80:83], v[216:219], v[48:51], v[80:83]\n"
  "v_mfma_f32_16x16x32_bf16 v[84:87], v[216:219], v[52:55], v[84:87]\n"
  "ds_read_b128 v[216:219], v247 offset:0\n"
  "s_waitcnt lgkmcnt(3)\n"
  "v_mfma_f32_16x16x32_bf16 v[80:83], v[220:223], v[56:59], v[80:83]\n"
  "v_mfma_f32_16x16x32_bf16 v[84:87], v[220:223], v[60:63], v[84:87]\n"
  "ds_read_b128 v[220:223], v247 offset:2048\n"
  "s_waitcnt lgkmcnt(3)\n"
  "v_mfma_f32_16x16x32_bf16 v[80:83], v[208:211], v[64:67], v[80:83]\n"
  "v_mfma_f32_16x16x32_bf16 v[84:87], v[208:211], v[68:71], v[84:87]\n"
  "ds_read_b128 v[208:211], v247 offset:4096\n"
  "s_waitcnt lgkmcnt(3)\n"
  "v_mfma_f32_16x16x32_bf16 v[80:83], v[212:215], v[72:75], v[80:83]\n"
  "v_mfma_f32_16x16x32_bf16 v[84:87], v[212:215], v[76:79], v[84:87]\n"
  "ds_read_b128 v[212:215], v247 offset:6144\n"
  "s_waitcnt lgkmcnt(3)\n"
  "v_mfma_f32_16x16x32_bf16 v[80:83], v[216:219], v[160:163], v[80:83]\n"
  "v_mfma_f32_16x16x32_bf16 v[84:87], v[216:219], v[164:167], v[84:87]\n"
  "s_waitcnt lgkmcnt(2)\n"
  "v_mfma_f32_16x16x32_bf16 v[80:83], v[220:223], v[168:171], v[80:83]\n"
  "v_mfma_f32_16x16x32_bf16 v[84:87], v[220:223], v[172:175], v[84:87]\n"
  "s_waitcnt lgkmcnt(1)\n"
  "v_mfma_f32_16x16x32_bf16 v[80:83], v[208:211], v[144:147], v[80:83]\n"
  "s_cmp_eq_u32 s62, 2\n"
  "s_cbranch_scc1 .Lk34_t8a\n"
  ".Lk34_t8a_r:\n"
  "v_mfma_f32_16x16x32_bf16 v[84:87], v[208:211], v[148:151], v[84:87]\n"
  "s_waitcnt lgkmcnt(0)\n"
  "v_mfma_f32_16x16x32_bf16 v[80:83], v[212:215], v[152:155], v[80:83]\n"
  "v_mfma_f32_16x16x32_bf16 v[84:87], v[212:215], v[156:159], v[84:87]\n"
  "s_add_u32 s69, s61, 0\n"
  "buffer_load_dwordx4 v[104:107], v240, s[36:39], s69 offen sc1\n"
  "s_add_u32 s69, s61, 0x2000\n"
  "buffer_load_dwordx4 v[108:111], v240, s[36:39], s69 offen sc1\n"
  "s_add_u32 s69, s61, 0x4000\n"
  "buffer_load_dwordx4 v[112:115], v240, s[36:39], s69 offen sc1\n"
  "s_waitcnt lgkmcnt(0)\n"
  "v_add_f32 v80, v80, v228\n"
  "v_add_f32 v81, v81, v229\n"
  "v_add_f32 v82, v82, v230\n"
  "v_add_f32 v83, v83, v231\n"
  "v_add_f32 v84, v84, v232\n"
  "v_add_f32 v85, v85, v233\n"
  "v_add_f32 v86, v86, v234\n"
  "v_add_f32 v87, v87, v235\n"
  "s_waitcnt vmcnt(0)\n"
  "v_max3_u32 v128, v104, v105, v106\n"
  "v_max3_u32 v129, v107, v108, v109\n"
  "v_max3_u32 v130, v110, v111, v112\n"
  "v_max3_u32 v131, v113, v114, v115\n"
  "v_max3_u32 v128, v128, v129, v130\n"
  "v_max_u32 v128, v128, v131\n"
  "v_cmp_eq_u32 vcc, -1, v128\n"
  "s_cbranch_vccnz .Lk34_inb\n"
  ".Lk34_inb_r:\n"
  "v_lshlrev_b32 v128, 16, v104\n"
  "v_and_b32 v129, 0xffff0000, v104\n"
  "v_add_f32 v80, v80, v128\n"
  "v_add_f32 v81, v81, v129\n"
  "v_lshlrev_b32 v130, 16, v105\n"
  "v_and_b32 v131, 0xffff0000, v105\n"
  "v_add_f32 v82, v82, v130\n"
  "v_add_f32 v83, v83, v131\n"
  "v_lshlrev_b32 v128, 16, v106\n"
  "v_and_b32 v129, 0xffff0000, v106\n"
  "v_add_f32 v84, v84, v128\n"
  "v_add_f32 v85, v85, v129\n"
  "v_lshlrev_b32 v130, 16, v107\n"
  "v_and_b32 v131, 0xffff0000, v107\n"
  "v_add_f32 v86, v86, v130\n"
  "v_add_f32 v87, v87, v131\n"
  "v_lshlrev_b32 v128, 16, v108\n"
  "v_and_b32 v129, 0xffff0000, v108\n"
  "v_add_f32 v80, v80, v128\n"
  "v_add_f32 v81, v81, v129\n"
  "v_lshlrev_b32 v130, 16, v109\n"
  "v_and_b32 v131, 0xffff0000, v109\n"
  "v_add_f32 v82, v82, v130\n"
  "v_add_f32 v83, v83, v131\n"
  "v_lshlrev_b32 v128, 16, v110\n"
  "v_and_b32 v129, 0xffff0000, v110\n"
  "v_add_f32 v84, v84, v128\n"
  "v_add_f32 v85, v85, v129\n"
  "v_lshlrev_b32 v130, 16, v111\n"
  "v_and_b32 v131, 0xffff0000, v111\n"
  "v_add_f32 v86, v86, v130\n"
  "v_add_f32 v87, v87, v131\n"
  "v_lshlrev_b32 v128, 16, v112\n"
  "v_and_b32 v129, 0xffff0000, v112\n"
  "v_add_f32 v80, v80, v128\n"
  "v_add_f32 v81, v81, v129\n"
  "v_lshlrev_b32 v130, 16, v113\n"
  "v_and_b32 v131, 0xffff0000, v113\n"
  "v_add_f32 v82, v82, v130\n"
  "v_add_f32 v83, v83, v131\n"
  "v_lshlrev_b32 v128, 16, v114\n"
  "v_and_b32 v129, 0xffff0000, v114\n"
  "v_add_f32 v84, v84, v128\n"
  "v_add_f32 v85, v85, v129\n"
  "v_lshlrev_b32 v130, 16, v115\n"
  "v_and_b32 v131, 0xffff0000, v115\n"
  "v_add_f32 v86, v86, v130\n"
  "v_add_f32 v87, v87, v131\n"
  "v_lshlrev_b32 v128, 16, v88\n"
  "v_and_b32 v129, 0xffff0000, v88\n"
  "v_lshlrev_b32 v130, 16, v89\n"
  "v_and_b32 v131, 0xffff0000, v89\n"
  "v_lshlrev_b32 v132, 16, v90\n"
  "v_and_b32 v133, 0xffff0000, v90\n"
  "v_lshlrev_b32 v134, 16, v91\n"
  "v_and_b32 v135, 0xffff0000, v91\n"
  "v_fmac_f32 v128, v92, v80\n"
  "v_fmac_f32 v129, v93, v81\n"
  "v_fmac_f32 v130, v94, v82\n"
  "v_fmac_f32 v131, v95, v83\n"
  "v_fmac_f32 v132, v92, v84\n"
  "v_fmac_f32 v133, v93, v85\n"
  "v_fmac_f32 v134, v94, v86\n"
  "v_fmac_f32 v135, v95, v87\n"
  "v_cvt_pk_bf16_f32 v80, v128, v129\n"
  "v_cvt_pk_bf16_f32 v81, v130, v131\n"
  "v_cvt_pk_bf16_f32 v82, v132, v133\n"
  "v_cvt_pk_bf16_f32 v83, v134, v135\n"
  "buffer_store_dwordx2 v[80:81], v245, s[40:43], s65 offen\n"
  "buffer_store_dwordx2 v[82:83], v245, s[40:43], s65 offen offset:1152\n"
  "s_cmp_lg_u32 s62, 2\n"
  "s_cbranch_scc1 .Lk34_end\n"
  "v_max3_u32 v128, v116, v117, v118\n"
  "v_max3_u32 v129, v119, v120, v121\n"
  "v_max3_u32 v130, v122, v123, v124\n"
  "v_max3_u32 v131, v125, v126, v127\n"
  "v_max3_u32 v128, v128, v129, v130\n"
  "v_max_u32 v128, v128, v131\n"
  "v_cmp_eq_u32 vcc, -1, v128\n"
  "s_cbranch_vccnz .Lk34_in8\n"
  ".Lk34_in8_r:\n"
  "s_lshl_b32 s69, s67, 5\n"
  "s_add_u32 s69, s69, s65\n"
  "v_lshlrev_b32 v128, 16, v116\n"
  "v_and_b32 v129, 0xffff0000, v116\n"
  "v_add_f32 v136, v136, v128\n"
  "v_add_f32 v137, v137, v129\n"
  "v_lshlrev_b32 v130, 16, v117\n"
  "v_and_b32 v131, 0xffff0000, v117\n"
  "v_add_f32 v138, v138, v130\n"
  "v_add_f32 v139, v139, v131\n"
  "v_lshlrev_b32 v128, 16, v118\n"
  "v_and_b32 v129, 0xffff0000, v118\n"
  "v_add_f32 v140, v140, v128\n"
  "v_add_f32 v141, v141, v129\n"
  "v_lshlrev_b32 v130, 16, v119\n"
  "v_and_b32 v131, 0xffff0000, v119\n"
  "v_add_f32 v142, v142, v130\n"
  "v_add_f32 v143, v143, v131\n"
  "v_lshlrev_b32 v128, 16, v120\n"
  "v_and_b32 v129, 0xffff0000, v120\n"
  "v_add_f32 v136, v136, v128\n"
  "v_add_f32 v137, v137, v129\n"
  "v_lshlrev_b32 v130, 16, v121\n"
  "v_and_b32 v131, 0xffff0000, v121\n"
  "v_add_f32 v138, v138, v130\n"
  "v_add_f32 v139, v139, v131\n"
  "v_lshlrev_b32 v128, 16, v122\n"
  "v_and_b32 v129, 0xffff0000, v122\n"
  "v_add_f32 v140, v140, v128\n"
  "v_add_f32 v141, v141, v129\n"
  "v_lshlrev_b32 v130, 16, v123\n"
  "v_and_b32 v131, 0xffff0000, v123\n"
  "v_add_f32 v142, v142, v130\n"
  "v_add_f32 v143, v143, v131\n"
  "v_lshlrev_b32 v128, 16, v124\n"
  "v_and_b32 v129, 0xffff0000, v124\n"
  "v_add_f32 v136, v136, v128\n"
  "v_add_f32 v137, v137, v129\n"
  "v_lshlrev_b32 v130, 16, v125\n"
  "v_and_b32 v131, 0xffff0000, v125\n"
  "v_add_f32 v138, v138, v130\n"
  "v_add_f32 v139, v139, v131\n"
  "v_lshlrev_b32 v128, 16, v126\n"
  "v_and_b32 v129, 0xffff0000, v126\n"
  "v_add_f32 v140, v140, v128\n"
  "v_add_f32 v141, v141, v129\n"
  "v_lshlrev_b32 v130, 16, v127\n"
  "v_and_b32 v131, 0xffff0000, v127\n"
  "v_add_f32 v142, v142, v130\n"
  "v_add_f32 v143, v143, v131\n"
  "v_lshlrev_b32 v128, 16, v96\n"
  "v_and_b32 v129, 0xffff0000, v96\n"
  "v_lshlrev_b32 v130, 16, v97\n"
  "v_and_b32 v131, 0xffff0000, v97\n"
  "v_lshlrev_b32 v132, 16, v98\n"
  "v_and_b32 v133, 0xffff0000, v98\n"
  "v_lshlrev_b32 v134, 16, v99\n"
  "v_and_b32 v135, 0xffff0000, v99\n"
  "v_fmac_f32 v128, v100, v136\n"
  "v_fmac_f32 v129, v101, v137\n"
  "v_fmac_f32 v130, v102, v138\n"
  "v_fmac_f32 v131, v103, v139\n"
  "v_fmac_f32 v132, v100, v140\n"
  "v_fmac_f32 v133, v101, v141\n"
  "v_fmac_f32 v134, v102, v142\n"
  "v_fmac_f32 v135, v103, v143\n"
  "v_cvt_pk_bf16_f32 v136, v128, v129\n"
  "v_cvt_pk_bf16_f32 v137, v130, v131\n"
  "v_cvt_pk_bf16_f32 v138, v132, v133\n"
  "v_cvt_pk_bf16_f32 v139, v134, v135\n"
  "buffer_store_dwordx2 v[136:137], v245, s[40:43], s69 offen\n"
  "buffer_store_dwordx2 v[138:139], v245, s[40:43], s69 offen offset:1152\n"
  "s_branch .Lk34_end\n"
  ".Lk34_stat:\n"
  "s_nop 7\n"
  "ds_read_b128 v[224:227], v242 offset:24576\n"
  "ds_read_b128 v[228:231], v242 offset:25600\n"
  "s_waitcnt lgkmcnt(0)\n"
  "v_add_f32 v224, v224, v228\n"
  "v_add_f32 v225, v225, v229\n"
  "v_add_f32 v226, v226, v230\n"
  "v_add_f32 v227, v227, v231\n"
  "v_add_f32_dpp v224, v224, v224 row_ror:4 row_mask:0xf bank_mask:0xf\n"
  "v_add_f32_dpp v225, v225, v225 row_ror:4 row_mask:0xf bank_mask:0xf\n"
  "v_add_f32_dpp v226, v226, v226 row_ror:4 row_mask:0xf bank_mask:0xf\n"
  "v_add_f32_dpp v227, v227, v227 row_ror:4 row_mask:0xf bank_mask:0xf\n"
  "v_add_f32_dpp v224, v224, v224 row_ror:8 row_mask:0xf bank_mask:0xf\n"
  "v_add_f32_dpp v225, v225, v225 row_ror:8 row_mask:0xf bank_mask:0xf\n"
  "v_add_f32_dpp v226, v226, v226 row_ror:8 row_mask:0xf bank_mask:0xf\n"
  "v_add_f32_dpp v227, v227, v227 row_ror:8 row_mask:0xf bank_mask:0xf\n"
  "v_mov_b32 v228, v224\n"
  "v_mov_b32 v229, v225\n"
  "v_mov_b32 v230, v226\n"
  "v_mov_b32 v231, v227\n"
  "v_permlane16_swap_b32 v224, v228\n"
  "v_permlane16_swap_b32 v225, v229\n"
  "v_permlane16_swap_b32 v226, v230\n"
  "v_permlane16_swap_b32 v227, v231\n"
  "v_add_f32 v224, v224, v228\n"
  "v_add_f32 v225, v225, v229\n"
  "v_add_f32 v226, v226, v230\n"
  "v_add_f32 v227, v227, v231\n"
  "v_mov_b32 v228, v224\n"
  "v_mov_b32 v229, v225\n"
  "v_mov_b32 v230, v226\n"
  "v_mov_b32 v231, v227\n"
  "v_permlane32_swap_b32 v224, v228\n"
  "v_permlane32_swap_b32 v225, v229\n"
  "v_permlane32_swap_b32 v226, v230\n"
  "v_permlane32_swap_b32 v227, v231\n"
  "v_add_f32 v224, v224, v228\n"
  "v_add_f32 v225, v225, v229\n"
  "v_add_f32 v226, v226, v230\n"
  "v_add_f32 v227, v227, v231\n"
  "v_mul_f32 v224, 0x3ae38e39, v224\n"
  "v_mul_f32 v225, 0x3ae38e39, v225\n"
  "v_mul_f32 v226, 0x3ae38e39, v226\n"
  "v_mul_f32 v227, 0x3ae38e39, v227\n"
  "v_fma_f32 v225, -v224, v224, v225\n"
  "v_fma_f32 v227, -v226, v226, v227\n"
  "v_max_f32 v225, 0, v225\n"
  "v_max_f32 v227, 0, v227\n"
  "v_cvt_pk_bf16_f32 v228, v224, v226\n"
  "v_lshlrev_b32 v224, 16, v228\n"
  "v_and_b32 v226, 0xffff0000, v228\n"
  "v_cvt_pk_bf16_f32 v228, v225, v227\n"
  "v_lshlrev_b32 v225, 16, v228\n"
  "v_and_b32 v227, 0xffff0000, v228\n"
  "v_add_f32 v225, 0x3727c5ac, v225\n"
  "v_add_f32 v227, 0x3727c5ac, v227\n"
  "v_cvt_pk_bf16_f32 v228, v225, v227\n"
  "v_lshlrev_b32 v225, 16, v228\n"
  "v_and_b32 v227, 0xffff0000, v228\n"
  "v_sqrt_f32 v225, v225\n"
  "v_sqrt_f32 v227, v227\n"
  "s_nop 0\n"
  "v_cvt_pk_bf16_f32 v228, v225, v227\n"
  "v_lshlrev_b32 v225, 16, v228\n"
  "v_and_b32 v227, 0xffff0000, v228\n"
  "v_rcp_f32 v232, v225\n"
  "v_rcp_f32 v234, v227\n"
  "v_mul_f32_e64 v233, -v232, v224\n"
  "v_mul_f32_e64 v235, -v234, v226\n"
  "v_min_u32 v232, -2, v232\n"
  "v_min_u32 v233, -2, v233\n"
  "v_min_u32 v234, -2, v234\n"
  "v_min_u32 v235, -2, v235\n"
  "s_mov_b64 exec, 15\n"
  "buffer_store_dwordx4 v[232:235], v240, s[36:39], s50 offen sc1\n"
  "s_mov_b64 exec, -1\n"
  "s_nop 2\n"
  "s_branch .Lk34_stat_r\n"
  ".Lk34_stp:\n"
  "s_nop 7\n"
  "s_mov_b32 s72, 0x20000\n"
  ".Lk34_stp_l:\n"
  "s_sleep 1\n"
  "buffer_load_dwordx4 v[224:227], v245, s[36:39], s49 offen sc1\n"
  "s_waitcnt vmcnt(0)\n"
  "v_max3_u32 v236, v224, v225, v226\n"
  "v_max_u32 v236, v236, v227\n"
  "v_cmp_eq_u32 vcc, -1, v236\n"
  "s_cbranch_vccz .Lk34_stp_d\n"
  "s_sub_u32 s72, s72, 1\n"
  "s_cmp_lg_u32 s72, 0\n"
  "s_cbranch_scc1 .Lk34_stp_l\n"
  ".Lk34_stp_d:\n"
  "s_nop 2\n"
  "s_branch .Lk34_stp_r\n"
  ".Lk34_pub220:\n"
  "s_nop 7\n"
  "v_mov_b32 v251, 5\n"
  "s_mov_b64 exec, 1\n"
  "buffer_store_dword v251, off, s[36:39], s54 sc1\n"
  "s_mov_b64 exec, -1\n"
  "s_nop 2\n"
  "s_branch .Lk34_pub220_r\n"
  ".Lk34_fl5:\n"
  "s_nop 7\n"
  "s_mov_b32 s72, 0x20000\n"
  ".Lk34_fl5_l:\n"
  "s_sleep 1\n"
  "buffer_load_dword v249, v250, s[36:39], s55 offen sc1\n"
  "s_waitcnt vmcnt(0)\n"
  "v_cmp_gt_u32 vcc, 5, v249\n"
  "s_cbranch_vccz .Lk34_fl5_d\n"
  "s_sub_u32 s72, s72, 1\n"
  "s_cmp_lg_u32 s72, 0\n"
  "s_cbranch_scc1 .Lk34_fl5_l\n"
  ".Lk34_fl5_d:\n"
  "s_nop 2\n"
  "s_branch .Lk34_fl5_r\n"
  ".Lk34_pub284:\n"
  "s_nop 7\n"
  "v_mov_b32 v251, 7\n"
  "s_mov_b64 exec, 1\n"
  "buffer_store_dword v251, off, s[36:39], s54 sc1\n"
  "s_mov_b64 exec, -1\n"
  "s_nop 2\n"
  "s_branch .Lk34_pub284_r\n"
  ".Lk34_fl7:\n"
  "s_nop 7\n"
  "s_mov_b32 s72, 0x20000\n"
  ".Lk34_fl7_l:\n"
  "s_sleep 1\n"
  "buffer_load_dword v249, v250, s[36:39], s55 offen sc1\n"
  "s_waitcnt vmcnt(0)\n"
  "v_cmp_gt_u32 vcc, 7, v249\n"
  "s_cbranch_vccz .Lk34_fl7_d\n"
  "s_sub_u32 s72, s72, 1\n"
  "s_cmp_lg_u32 s72, 0\n"
  "s_cbranch_scc1 .Lk34_fl7_l\n"
  ".Lk34_fl7_d:\n"
  "s_nop 2\n"
  "s_branch .Lk34_fl7_r\n"
  ".Lk34_pub364:\n"
  "s_nop 7\n"
  "v_mov_b32 v251, 9\n"
  "s_mov_b64 exec, 1\n"
  "buffer_store_dword v251, off, s[36:39], s54 sc1\n"
  "s_mov_b64 exec, -1\n"
  "s_nop 2\n"
  "s_branch .Lk34_pub364_r\n"
  ".Lk34_fl9:\n"
  "s_nop 7\n"
  "s_mov_b32 s72, 0x20000\n"
  ".Lk34_fl9_l:\n"
  "s_sleep 1\n"
  "buffer_load_dword v249, v250, s[36:39], s55 offen sc1\n"
  "s_waitcnt vmcnt(0)\n"
  "v_cmp_gt_u32 vcc, 9, v249\n"
  "s_cbranch_vccz .Lk34_fl9_d\n"
  "s_sub_u32 s72, s72, 1\n"
  "s_cmp_lg_u32 s72, 0\n"
  "s_cbranch_scc1 .Lk34_fl9_l\n"
  ".Lk34_fl9_d:\n"
  "s_nop 2\n"
  "s_branch .Lk34_fl9_r\n"
  ".Lk34_pf:\n"
  "s_nop 7\n"
  "s_mov_b32 s72, 0x20000\n"
  ".Lk34_pf_l:\n"
  "s_sleep 1\n"
  "buffer_load_dword v249, v250, s[36:39], s73 offen sc1\n"
  "s_waitcnt vmcnt(0)\n"
  "v_cmp_gt_u32 vcc, 5, v249\n"
  "s_cbranch_vccz .Lk34_pf_d\n"
  "s_sub_u32 s72, s72, 1\n"
  "s_cmp_lg_u32 s72, 0\n"
  "s_cbranch_scc1 .Lk34_pf_l\n"
  ".Lk34_pf_d:\n"
  "s_nop 2\n"
  "s_branch .Lk34_pf_r\n"
  ".Lk34_t8a:\n"
  "s_nop 7\n"
  "s_lshl_b32 s69, s67, 16\n"
  "s_add_u32 s69, s69, s63\n"
  "buffer_load_dwordx4 v[96:99], v243, s[36:39], s69 offen\n"
  "s_lshl_b32 s69, s67, 6\n"
  "s_add_u32 s69, s69, s64\n"
  "buffer_load_dwordx4 v[100:103], v244, s[36:39], s69 offen\n"
  "s_add_u32 s69, s66, 0\n"
  "buffer_load_dwordx4 v[116:119], v240, s[36:39], s69 offen sc1\n"
  "s_add_u32 s69, s66, 0x400\n"
  "buffer_load_dwordx4 v[120:123], v240, s[36:39], s69 offen sc1\n"
  "s_add_u32 s69, s66, 0x800\n"
  "buffer_load_dwordx4 v[124:127], v240, s[36:39], s69 offen sc1\n"
  "s_nop 2\n"
  "s_branch .Lk34_t8a_r\n"
  ".Lk34_inb:\n"
  "s_nop 7\n"
  "s_mov_b32 s72, 0x20000\n"
  ".Lk34_inb_l:\n"
  "s_sleep 1\n"
  "s_add_u32 s69, s61, 0\n"
  "buffer_load_dwordx4 v[104:107], v240, s[36:39], s69 offen sc1\n"
  "s_add_u32 s69, s61, 0x2000\n"
  "buffer_load_dwordx4 v[108:111], v240, s[36:39], s69 offen sc1\n"
  "s_add_u32 s69, s61, 0x4000\n"
  "buffer_load_dwordx4 v[112:115], v240, s[36:39], s69 offen sc1\n"
  "s_add_u32 s69, s66, 0\n"
  "buffer_load_dwordx4 v[116:119], v240, s[36:39], s69 offen sc1\n"
  "s_add_u32 s69, s66, 0x400\n"
  "buffer_load_dwordx4 v[120:123], v240, s[36:39], s69 offen sc1\n"
  "s_add_u32 s69, s66, 0x800\n"
  "buffer_load_dwordx4 v[124:127], v240, s[36:39], s69 offen sc1\n"
  "s_waitcnt vmcnt(0)\n"
  "v_max3_u32 v128, v104, v105, v106\n"
  "v_max3_u32 v129, v107, v108, v109\n"
  "v_max3_u32 v130, v110, v111, v112\n"
  "v_max3_u32 v131, v113, v114, v115\n"
  "v_max3_u32 v128, v128, v129, v130\n"
  "v_max_u32 v128, v128, v131\n"
  "v_cmp_eq_u32 vcc, -1, v128\n"
  "s_cbranch_vccz .Lk34_inb_d\n"
  "s_sub_u32 s72, s72, 1\n"
  "s_cmp_lg_u32 s72, 0\n"
  "s_cbranch_scc1 .Lk34_inb_l\n"
  ".Lk34_inb_d:\n"
  "s_nop 2\n"
  "s_branch .Lk34_inb_r\n"
  ".Lk34_in8:\n"
  "s_nop 7\n"
  "s_mov_b32 s72, 0x20000\n"
  ".Lk34_in8_l:\n"
  "s_sleep 1\n"
  "s_add_u32 s69, s66, 0\n"
  "buffer_load_dwordx4 v[116:119], v240, s[36:39], s69 offen sc1\n"
  "s_add_u32 s69, s66, 0x400\n"
  "buffer_load_dwordx4 v[120:123], v240, s[36:39], s69 offen sc1\n"
  "s_add_u32 s69, s66, 0x800\n"
  "buffer_load_dwordx4 v[124:127], v240, s[36:39], s69 offen sc1\n"
  "s_waitcnt vmcnt(0)\n"
  "v_max3_u32 v128, v116, v117, v118\n"
  "v_max3_u32 v129, v119, v120, v121\n"
  "v_max3_u32 v130, v122, v123, v124\n"
  "v_max3_u32 v131, v125, v126, v127\n"
  "v_max3_u32 v128, v128, v129, v130\n"
  "v_max_u32 v128, v128, v131\n"
  "v_cmp_eq_u32 vcc, -1, v128\n"
  "s_cbranch_vccz .Lk34_in8_d\n"
  "s_sub_u32 s72, s72, 1\n"
  "s_cmp_lg_u32 s72, 0\n"
  "s_cbranch_scc1 .Lk34_in8_l\n"
  ".Lk34_in8_d:\n"
  "s_nop 2\n"
  "s_branch .Lk34_in8_r\n"
  ".Lk34_end:\n"
      : "+{v240}"(vl)
      : "{s[36:51]}"(A), "{s[52:67]}"(B)
      :
        "v0", "v1", "v2", "v3", "v4", "v5", "v6", "v7", "v8", "v9", "v10", "v11", "v12", "v13", "v14", "v15", "v16", "v17",
        "v18", "v19", "v20", "v21", "v22", "v23", "v24", "v25", "v26", "v27", "v28", "v29", "v30", "v31", "v32", "v33", "v34",
        "v35", "v36", "v37", "v38", "v39", "v40", "v41", "v42", "v43", "v44", "v45", "v46", "v47", "v48", "v49", "v50", "v51",
        "v52", "v53", "v54", "v55", "v56", "v57", "v58", "v59", "v60", "v61", "v62", "v63", "v64", "v65", "v66", "v67", "v68",
        "v69", "v70", "v71", "v72", "v73", "v74", "v75", "v76", "v77", "v78", "v79", "v80", "v81", "v82", "v83", "v84", "v85",
        "v86", "v87", "v88", "v89", "v90", "v91", "v92", "v93", "v94", "v95", "v96", "v97", "v98", "v99", "v100", "v101",
        "v102", "v103", "v104", "v105", "v106", "v107", "v108", "v109", "v110", "v111", "v112", "v113", "v114", "v115", "v116",
        "v117", "v118", "v119", "v120", "v121", "v122", "v123", "v124", "v125", "v126", "v127", "v128", "v129", "v130", "v131",
        "v132", "v133", "v134", "v135", "v136", "v137", "v138", "v139", "v140", "v141", "v142", "v143", "v144", "v145", "v146",
        "v147", "v148", "v149", "v150", "v151", "v152", "v153", "v154", "v155", "v156", "v157", "v158", "v159", "v160", "v161",
        "v162", "v163", "v164", "v165", "v166", "v167", "v168", "v169", "v170", "v171", "v172", "v173", "v174", "v175", "v176",
        "v177", "v178", "v179", "v180", "v181", "v182", "v183", "v184", "v185", "v186", "v187", "v188", "v189", "v190", "v191",
        "v192", "v193", "v194", "v195", "v196", "v197", "v198", "v199", "v200", "v201", "v202", "v203", "v204", "v205", "v206",
        "v207", "v208", "v209", "v210", "v211", "v212", "v213", "v214", "v215", "v216", "v217", "v218", "v219", "v220", "v221",
        "v222", "v223", "v224", "v225", "v226", "v227", "v228", "v229", "v230", "v231", "v232", "v233", "v234", "v235", "v236",
        "v237", "v238", "v239", "v241", "v242", "v243", "v244", "v245", "v246", "v247", "v248", "v249", "v250", "v251", "v252",
        "v253", "v254", "v255", "s68", "s69", "s70", "s71", "s72", "s73", "s74", "s75", "vcc", "scc", "memory");
  STAMP_END3(1);
}

extern "C" void kernel_launch(void* const* d_in, const int* in_sizes, int n_in, void* d_out, int out_size, void* d_ws, size_t ws_size,
                              hipStream_t stream) {
  const u16* const* in = (const u16* const*)d_in;
  char* ws = (char*)d_ws;
  k1<<<256, 512, 0, stream>>>(in[0], in[1], in[2], in[3], in[4], in[5], in[6], in[7], in[8], in[13], in[14], in[10], in[11], in[12], ws);
  k3<<<256, 512, 0, stream>>>(ws, (u16*)d_out);
}
```
